```python
import torch

def _softmax(x, axis=-1):
    return torch.softmax(x, dim=axis)
def _gelu(x):
    return torch.nn.functional.gelu(x, approximate="tanh")
def _silu(x):
    return torch.nn.functional.silu(x)
def _sigmoid(x):
    return torch.sigmoid(x)
def _layernorm(x, axis=-1, eps=1e-5):
    m = x.mean(dim=axis, keepdim=True)
    v = x.var(dim=axis, keepdim=True, unbiased=False)
    return (x - m) / torch.sqrt(v + eps)
def _scatter_add(tgt, idx, src, axis=0):
    return tgt.clone().index_add_(axis, idx, src)
def _segment_sum(src, seg, n):
    out = torch.zeros((n,) + src.shape[1:], dtype=src.dtype)
    return out.index_add_(0, seg, src)
def _pad(x, pw):
    flat = tuple(v for pair in reversed(pw) for v in pair)
    return torch.nn.functional.pad(x, flat)

kernel_name = 'gated_mix__ssm_conv_s4__dense_activation_squared_relu__prompt'

def setup_inputs():

    _w = (lambda a: (lambda t: t.to(torch.bfloat16) if t.is_floating_point() else t)(torch.as_tensor(a)))
    def _A_setup():
        B, L, D, N = 1, 2048, 512, 128
        A_re = -(lambda _x: torch.exp(torch.as_tensor(_x)))((lambda *s: torch.randn(*s, dtype=torch.bfloat16) * 3.2)(D, N))
        A_im = torch.pi * (lambda *s: torch.randn(*s, dtype=torch.bfloat16) * 0.1)(D, N)
        B_re = (lambda *s: torch.randn(*s, dtype=torch.bfloat16) * 0.2)(D, N)
        B_im = (lambda *s: torch.randn(*s, dtype=torch.bfloat16) * 0.2)(D, N)
        C_re = (lambda *s: torch.randn(*s, dtype=torch.bfloat16) * 0.2)(D, N)
        C_im = (lambda *s: torch.randn(*s, dtype=torch.bfloat16) * 0.2)(D, N)
        log_dt = (lambda *s: torch.randn(*s, dtype=torch.bfloat16) * 0.1)(D) * 0.5 - 4.0
        Dskip = (lambda *s: torch.randn(*s, dtype=torch.bfloat16) * 0.1)(D)
        return {
            "u": _w((lambda *s: torch.randn(*s, dtype=torch.bfloat16) * 0.05)(B, L, D)),
            "A_re": _w(A_re),
            "A_im": _w(A_im),
            "B_re": _w(B_re),
            "B_im": _w(B_im),
            "C_re": _w(C_re),
            "C_im": _w(C_im),
            "log_dt": _w(log_dt),
            "Dskip": _w(Dskip),
        }
    _A_in = _A_setup()

    def _B_setup():
        B, S, D, F = 1, 2048, 512, 1376
        return {
            "x": _w((lambda *s: torch.randn(*s, dtype=torch.bfloat16) * 0.05)(B, S, D)),
            "w1": _w((lambda *s: torch.randn(*s, dtype=torch.bfloat16) * 0.05)(D, F)),
            "b1": _w((lambda *s: torch.randn(*s, dtype=torch.bfloat16) * 0.05)(F)),
            "w2": _w((lambda *s: torch.randn(*s, dtype=torch.bfloat16) * 0.05)(F, D)),
            "b2": _w((lambda *s: torch.randn(*s, dtype=torch.bfloat16) * 0.05)(D)),
        }
    _B_in = _B_setup()

    return {
        "A_u": _A_in["u"],
        "A_A_re": _A_in["A_re"],
        "A_A_im": _A_in["A_im"],
        "A_B_re": _A_in["B_re"],
        "A_B_im": _A_in["B_im"],
        "A_C_re": _A_in["C_re"],
        "A_C_im": _A_in["C_im"],
        "A_log_dt": _A_in["log_dt"],
        "A_Dskip": _A_in["Dskip"],
        "B_x": _B_in["x"],
        "B_w1": _B_in["w1"],
        "B_b1": _B_in["b1"],
        "B_w2": _B_in["w2"],
        "B_b2": _B_in["b2"],
        "gate_w": _w((lambda *s: torch.randn(*s, dtype=torch.bfloat16) * 0.05)(512) * 16.0),
    }

def reference(A_u, A_A_re, A_A_im, A_B_re, A_B_im, A_C_re, A_C_im, A_log_dt, A_Dskip, B_x, B_w1, B_b1, B_w2, B_b2, gate_w):
    def _A_ref(u, A_re, A_im, B_re, B_im, C_re, C_im, log_dt, Dskip):
        Bsz, L, D = u.shape
        dt = torch.exp(log_dt)[:, None]
        A = A_re + 1j * A_im
        Bc = B_re + 1j * B_im
        Cc = C_re + 1j * C_im
        logdA = dt * A
        dB = dt * Bc
        l = (lambda x, dt: x.to(dt))(torch.arange(L), A_re.dtype)
        P = torch.exp(logdA[:, :, None] * l[None, None, :])
        K = torch.einsum("dn,dnl,dn->dl", Cc, P, dB).real
        L2 = 2 * L
        Kp = _pad(K, ((0, 0), (0, L)))
        up = (lambda x, *d: x.permute(*d))(u, 0, 2, 1)
        up = _pad(up, ((0, 0), (0, 0), (0, L)))
        Kf = (lambda x, axes=(-2,-1): torch.fft.rfft2(x.float(), dim=axes))(Kp[:, None, :], axes=(-2, -1))
        Uf = (lambda x, axes=(-2,-1): torch.fft.rfft2(x.float(), dim=axes))(up[:, :, None, :], axes=(-2, -1))
        Yf = Uf * Kf[None, :, :, :]
        y = (lambda x, s, axes=(-2,-1): torch.fft.irfft2(x, s=s, dim=axes).to(torch.bfloat16))(Yf, (1, L2), axes=(-2, -1))[:, :, 0, :L]
        y = (lambda x, *d: x.permute(*d))(y, 0, 2, 1) + u * Dskip[None, None, :]
        return y

    def _B_ref(x, w1, b1, w2, b2):
        h = torch.einsum("bsd,df->bsf", x, w1) + b1
        zero = torch.zeros_like(h)
        r = torch.where(h > zero, h, zero)
        h = r * r
        out = torch.einsum("bsf,fd->bsd", h, w2) + b2
        return out

    x = A_u
    g = _sigmoid((x * gate_w).sum(-1, keepdims=True))
    a = _A_ref(x, A_A_re, A_A_im, A_B_re, A_B_im, A_C_re, A_C_im, A_log_dt, A_Dskip)
    b = _B_ref(x, B_w1, B_b1, B_w2, B_b2)
    return g * a + (1 - g) * b
```

```cpp
#include <hip/hip_runtime.h>
#include <hip/hip_bf16.h>
#include <stdint.h>

typedef unsigned short u16;
constexpr int L = 2048, D = 512, N = 128, F = 1376;

__device__ __forceinline__ float bf2f(u16 x) { return __builtin_bit_cast(float, (unsigned)x << 16); }
__device__ __forceinline__ u16 f2bf(float f) { __hip_bfloat16 b = __float2bfloat16(f); return __builtin_bit_cast(u16, b); }
__device__ __forceinline__ float rbf(float f) { return bf2f(f2bf(f)); }

__global__ void kgen(const u16* Are, const u16* Aim, const u16* Bre, const u16* Bim, const u16* Cre, const u16* Cim,
                     const u16* logdt, float* K) {
  __shared__ float zr[N], zi[N], cr[N], ci[N];
  int d = blockIdx.x, t = threadIdx.x;
  float dt = rbf(expf(bf2f(logdt[d])));
  if (t < N) {
    int i = d * N + t;
    zr[t] = dt * bf2f(Are[i]); zi[t] = dt * bf2f(Aim[i]);
    float br = dt * bf2f(Bre[i]), bi = dt * bf2f(Bim[i]);
    float xr = bf2f(Cre[i]), xi = bf2f(Cim[i]);
    cr[t] = xr * br - xi * bi; ci[t] = xr * bi + xi * br;
  }
  __syncthreads();
  for (int j = t; j < L; j += blockDim.x) {
    float l = rbf((float)j);
    float s = 0.f;
    for (int n = 0; n < N; ++n) {
      float e = __expf(zr[n] * l), sn, cs;
      __sincosf(zi[n] * l, &sn, &cs);
      s += e * (cr[n] * cs - ci[n] * sn);
    }
    K[d * L + j] = s;
  }
}

__global__ void conv(const u16* u, const float* K, u16* y) {
  __shared__ float ks[L], us[L];
  int d = blockIdx.x, t = threadIdx.x;
  for (int j = t; j < L; j += blockDim.x) { ks[j] = K[d * L + j]; us[j] = bf2f(u[j * D + d]); }
  __syncthreads();
  for (int l = t; l < L; l += blockDim.x) {
    float s = 0.f;
    for (int j = 0; j <= l; ++j) s += ks[j] * us[l - j];
    y[l * D + d] = f2bf(s);
  }
}

__global__ void gemm1(const u16* x, const u16* w1, const u16* b1, u16* h) {
  __shared__ float xs[8][D];
  int t0 = blockIdx.x * 8, t = threadIdx.x;
  for (int i = t; i < 8 * D; i += blockDim.x) xs[i / D][i % D] = bf2f(x[t0 * D + i]);
  __syncthreads();
  for (int f = t; f < F; f += blockDim.x) {
    float s[8] = {};
    for (int k = 0; k < D; ++k) { float w = bf2f(w1[k * F + f]);
#pragma unroll
      for (int r = 0; r < 8; ++r) s[r] += xs[r][k] * w; }
#pragma unroll
    for (int r = 0; r < 8; ++r) { float v = rbf(rbf(s[r]) + bf2f(b1[f])); v = v > 0.f ? v : 0.f; h[(t0 + r) * F + f] = f2bf(v * v); }
  }
}

__global__ void gemm2(const u16* x, const u16* h, const u16* w2, const u16* b2, const u16* y, const u16* Dskip, const u16* gw, u16* out) {
  __shared__ float hs[8][F];
  __shared__ float gs[8];
  int t0 = blockIdx.x * 8, t = threadIdx.x;
  for (int i = t; i < 8 * F; i += blockDim.x) hs[i / F][i % F] = bf2f(h[t0 * F + i]);
  if (t < 8) { float s = 0.f; for (int k = 0; k < D; ++k) s += rbf(bf2f(x[(t0 + t) * D + k]) * bf2f(gw[k])); s = rbf(s); gs[t] = rbf(1.f / (1.f + expf(-s))); }
  __syncthreads();
  for (int c = t; c < D; c += blockDim.x) {
    float s[8] = {};
    for (int k = 0; k < F; ++k) { float w = bf2f(w2[k * D + c]);
#pragma unroll
      for (int r = 0; r < 8; ++r) s[r] += hs[r][k] * w; }
#pragma unroll
    for (int r = 0; r < 8; ++r) {
      float b = rbf(rbf(s[r]) + bf2f(b2[c]));
      float a = rbf(bf2f(y[(t0 + r) * D + c]) + rbf(bf2f(x[(t0 + r) * D + c]) * bf2f(Dskip[c])));
      float g = gs[r];
      out[(t0 + r) * D + c] = f2bf(rbf(g * a) + rbf(rbf(1.f - g) * b));
    }
  }
}

extern "C" void kernel_launch(void* const* d_in, const int* in_sizes, int n_in, void* d_out, int out_size, void* d_ws, size_t ws_size, hipStream_t stream) {
  const u16* u = (const u16*)d_in[0];
  float* K = (float*)d_ws;
  u16* y = (u16*)((char*)d_ws + (4 << 20));
  u16* h = (u16*)((char*)d_ws + (8 << 20));
  kgen<<<D, 256, 0, stream>>>((const u16*)d_in[1], (const u16*)d_in[2], (const u16*)d_in[3], (const u16*)d_in[4], (const u16*)d_in[5], (const u16*)d_in[6], (const u16*)d_in[7], K);
  conv<<<D, 256, 0, stream>>>(u, K, y);
  gemm1<<<L / 8, 256, 0, stream>>>(u, (const u16*)d_in[10], (const u16*)d_in[11], h);
  gemm2<<<L / 8, 256, 0, stream>>>(u, h, (const u16*)d_in[12], (const u16*)d_in[13], y, (const u16*)d_in[8], (const u16*)d_in[14], (u16*)d_out);
}
```

```cpp
#include <hip/hip_runtime.h>
#include <stdint.h>
#include <type_traits>

typedef unsigned short u16;
typedef unsigned u32;
typedef unsigned long long u64;
typedef u32 v4u __attribute__((ext_vector_type(4)));
typedef u32 v2u __attribute__((ext_vector_type(2)));
typedef float v2f __attribute__((ext_vector_type(2)));
typedef float f32x4 __attribute__((ext_vector_type(4)));
typedef float f32x16 __attribute__((ext_vector_type(16)));
typedef __bf16 bf16x2 __attribute__((ext_vector_type(2)));
typedef __bf16 bf16x8 __attribute__((ext_vector_type(8)));
typedef short v4s __attribute__((ext_vector_type(4)));
#define GAS __attribute__((address_space(1)))
#define LAS __attribute__((address_space(3)))
typedef __amdgpu_buffer_rsrc_t rsrc_t;

constexpr int L = 2048, D = 512, NM = 128, F = 1376;
constexpr int LDS_BYTES = 81920;
constexpr int SC1 = 16;
constexpr u32 WS_EPOCH = 0, WS_PF = 4096, WS_HF = 8192, WS_AF = 12288, WS_G = 16384;
constexpr u32 WS_UT = 1u << 20, WS_AT = 3u << 20, WS_H = 5u << 20, H_BYTES = 64u * 86u * 1024u;
constexpr u32 WS_STAMP = 16u << 20;
constexpr int SPIN_CAP = 200000;

struct Params {
  const u16 *u, *Are, *Aim, *Bre, *Bim, *Cre, *Cim, *logdt, *Dskip, *w1, *b1, *w2, *b2, *gw;
  u16* out;
  unsigned char* ws;
};

template <int I, int N, class Fn>
__device__ __forceinline__ void static_for(Fn&& f) {
  if constexpr (I < N) { f(std::integral_constant<int, I>{}); static_for<I + 1, N>(f); }
}
__device__ __forceinline__ float bf2f(u32 x) { return __builtin_bit_cast(float, x << 16); }
__device__ __forceinline__ float bfhi(u32 x) { return __builtin_bit_cast(float, x & 0xffff0000u); }
__device__ __forceinline__ u32 pkbf(float a, float b) {
  v2f v = {a, b};
  return __builtin_bit_cast(u32, __builtin_convertvector(v, bf16x2));
}
__device__ __forceinline__ float dot2bf(u32 a, u32 b, float c) {
  return __builtin_amdgcn_fdot2_f32_bf16(__builtin_bit_cast(bf16x2, a), __builtin_bit_cast(bf16x2, b), c, false);
}
__device__ __forceinline__ float rbf(float a) { return bf2f(pkbf(a, a) & 0xffffu); }
__device__ __forceinline__ rsrc_t mkrs(const void* p, u32 bytes) { return __builtin_amdgcn_make_buffer_rsrc((void*)p, 0, bytes, 0x00020000); }
__device__ __forceinline__ f32x16 mfma32(v4u a, v4u b, f32x16 c) {
  return __builtin_amdgcn_mfma_f32_32x32x16_bf16(__builtin_bit_cast(bf16x8, a), __builtin_bit_cast(bf16x8, b), c, 0, 0, 0);
}
__device__ __forceinline__ f32x4 mfma16(v4u a, v4u b, f32x4 c) {
  return __builtin_amdgcn_mfma_f32_16x16x32_bf16(__builtin_bit_cast(bf16x8, a), __builtin_bit_cast(bf16x8, b), c, 0, 0, 0);
}
__device__ __forceinline__ v2u trread(LAS unsigned char* p) {
  return __builtin_bit_cast(v2u, __builtin_amdgcn_ds_read_tr16_b64_v4i16((LAS v4s*)p));
}
__device__ __forceinline__ v4u cat(v2u a, v2u b) { return v4u{a.x, a.y, b.x, b.y}; }
__device__ __forceinline__ void dma16(u32 m0v, u32 voff, rsrc_t rs, u32 soff) {
  asm volatile("s_mov_b32 m0, %0\n\ts_nop 0\n\tbuffer_load_dwordx4 %1, %2, %3 offen lds" ::"s"(m0v), "v"(voff), "s"(rs), "s"(soff) : "memory");
}
__device__ __forceinline__ void st128_sc1(v4u v, rsrc_t rs, u32 voff, u32 soff) {
  __builtin_amdgcn_raw_buffer_store_b128(v, rs, voff, soff, SC1);
  asm volatile("s_nop 1" ::"v"(v) : "memory");
}
__device__ __forceinline__ float relu(float x) { return __builtin_bit_cast(float, max(__builtin_bit_cast(int, x), 0)); }
template <int N> __device__ __forceinline__ void wait_vm() { asm volatile("s_waitcnt vmcnt(%0)" ::"n"(N) : "memory"); }
__device__ __forceinline__ void wait_lgkm0() { asm volatile("s_waitcnt lgkmcnt(0)" ::: "memory"); }
__device__ __forceinline__ void raw_barrier() { asm volatile("s_waitcnt lgkmcnt(0)\n\ts_barrier" ::: "memory"); }
#define SCHED() __builtin_amdgcn_sched_barrier(0)

__device__ __forceinline__ u64 ld_flag(const unsigned char* ws, u32 off) {
  return __hip_atomic_load((const GAS u64*)(ws + off), __ATOMIC_RELAXED, __HIP_MEMORY_SCOPE_AGENT);
}
__device__ __forceinline__ void st_flag(unsigned char* ws, u32 off, u64 v) {
  __hip_atomic_store((GAS u64*)(ws + off), v, __ATOMIC_RELAXED, __HIP_MEMORY_SCOPE_AGENT);
}
__device__ __forceinline__ void wait_flags(const unsigned char* ws, u32 off, u64 tag) {
#pragma unroll 1
  for (int spin = 0; spin < SPIN_CAP; ++spin) {
    const u64 v = ld_flag(ws, off);
    if (__all(v == tag)) break;
    __builtin_amdgcn_s_sleep(2);
  }
  __builtin_amdgcn_fence(__ATOMIC_ACQUIRE, "wavefront");
  asm volatile("" ::: "memory");
}

#define STAMP(i)
#define STAMP_DECL
#define STAMP_OUT()

constexpr int G1_W = 32768;
__host__ __device__ constexpr int w2_stride(int n) { return (n >= 6 && n < 10) || (n >= 13 && n < 17) ? 8192 : 3072; }
__host__ __device__ constexpr int w2_off(int n) {
  return n < 3 ? G1_W + 36864 + 1024 * n : n < 6 ? G1_W + 24576 + 1024 * (n - 3) : n < 10 ? 1024 * (n - 6)
       : n < 13 ? G1_W + 1024 * (n - 10) : n < 17 ? 4096 + 1024 * (n - 13) : G1_W + 12288 + 1024 * (n - 17);
}
#define A_CHECK 15
#define ROT_EXPR ((j + 2 * i128) & 7)
__device__ __forceinline__ void ffn_role(const Params& p, LAS unsigned char* sm, const int g, const int r) {
  STAMP_DECL;
  STAMP(0);
  const int lane = threadIdx.x & 63, w = __builtin_amdgcn_readfirstlane(threadIdx.x >> 6);
  const int hh = lane >> 5, l31 = lane & 31;
  const u32 lds0 = (u32)(size_t)sm;
  const int i128 = 4 * (g >> 1) + (r >> 3), j = 8 * (g & 1) + (r & 7);
  const rsrc_t rsX = mkrs(p.u, L * D * 2), rsH = mkrs(p.ws + WS_H, H_BYTES);
  const v4u ones = {hh == 0 ? 0x3F80u : 0u, 0u, 0u, 0u};
  u64 ep;
  const int t64 = 8 * (g >> 1) + (r >> 2), c64 = 4 * (g & 1) + (r & 3);
  const rsrc_t rsW2 = mkrs(p.w2, F * D * 2);
  const u32 vw2 = (u32)((lane >> 3) * 1024 + (((lane & 7) ^ (((lane >> 4) & 1) << 2)) * 16));
  auto pieceW2 = [&](int n, int place) {
    const int row = min(352 * w + 8 * n, F - 8);
    dma16(lds0 + w * w2_stride(place) + w2_off(place), vw2, rsW2, (u32)(row * 1024 + c64 * 128));
  };
  if (j < 15) {
    const rsrc_t rsW1 = mkrs(p.w1, D * F * 2), rsB1 = mkrs(p.b1, F * 2);
    const int rot = ROT_EXPR;
    const u32 vx0 = (u32)((32 * w + (lane >> 3)) * 1024 + (((lane & 7) ^ (lane >> 4)) * 16)), vx1 = vx0 ^ 64u;
    u32 vw[3];
#pragma unroll
    for (int i = 0; i < 3; ++i) { const int b = 1024 * (3 * w + i) + 16 * lane; vw[i] = (u32)((b / 192) * (F * 2) + b % 192); }
    const u32 sx = (u32)(i128 * 128 * 1024), sw = (u32)(j * 192);
    auto pieceX = [&](int s, int q) {
      dma16(lds0 + w * 8192 + (s & 1) * 4096 + q * 1024, (q & 1) ? vx1 : vx0, rsX, sx + q * 8192 + (u32)((s + rot) & 7) * 128);
    };
    auto pieceW = [&](int s, int i) {
      dma16(lds0 + G1_W + (s % 3) * 12288 + (3 * w + i) * 1024, vw[i], rsW1, sw + (u32)((s + rot) & 7) * (64 * F * 2));
    };
#pragma unroll
    for (int s = 0; s < 2; ++s) {
#pragma unroll
      for (int q = 0; q < 4; ++q) pieceX(s, q);
#pragma unroll
      for (int i = 0; i < 3; ++i) pieceW(s, i);
    }
    u32 bv[3];
    {
      const u32 bo = (u32)((96 * j + l31) * 2);
      asm volatile("buffer_load_ushort %0, %4, %5, 0 offen\n\tbuffer_load_ushort %1, %4, %5, 0 offen offset:64\n\t"
                   "buffer_load_ushort %2, %4, %5, 0 offen offset:128\n\tglobal_load_dwordx2 %3, %6, %7 sc1"
                   : "=&v"(bv[0]), "=&v"(bv[1]), "=&v"(bv[2]), "=&v"(ep) : "v"(bo), "s"(rsB1), "v"(0u), "s"(p.ws) : "memory");
    }
    LAS unsigned char* xa[4];
#pragma unroll
    for (int ks = 0; ks < 4; ++ks) xa[ks] = sm + w * 8192 + l31 * 128 + (((2 * ks + hh) ^ ((l31 >> 1) & 7)) * 16);
    LAS unsigned char* wa = sm + G1_W + (8 * hh + ((lane & 15) >> 2)) * 192 + 32 * ((lane >> 4) & 1) + 8 * (lane & 3);
    f32x16 acc[3] = {};
    v4u xf[4], wf[2][3];
    auto readW = [&](int s, int ks, v4u (&dst)[3]) {
#pragma unroll
      for (int ft = 0; ft < 3; ++ft) {
        LAS unsigned char* a = wa + (s % 3) * 12288 + 16 * ks * 192 + 64 * ft;
        dst[ft] = cat(trread(a), trread(a + 4 * 192));
      }
    };
    static_for<0, 9>([&](auto ic) {
      constexpr int s = decltype(ic)::value;
      auto slot = [&](auto kc) {
        constexpr int k = decltype(kc)::value;
        if constexpr (s <= 5) {
          if constexpr (k < 3) pieceW(s + 2, k);
          else if constexpr (k >= 3 && k < 7) pieceX(s + 2, k - 3);
        } else if constexpr (s == 6) {
          if constexpr (k < 10) pieceW2(k, k);
        } else if constexpr (s == 7) {
          if constexpr (k < 7) pieceW2(10 + k, 10 + k);
        } else {
          if constexpr (k < 3) pieceW2(17 + k, 17 + k);
        }
        SCHED();
      };
      constexpr int NW = s <= 1 ? 11 : s <= 6 ? 7 : 10;
      if constexpr (s < 8) wait_vm<NW>();
      raw_barrier();
      if constexpr (s == 0) STAMP(1);
      if constexpr (s == 2) STAMP(14);
      if constexpr (s == 6) STAMP(15);
      if constexpr (s == 8) STAMP(2);
      if constexpr (s == 2) asm volatile("" : "+v"(bv[0]), "+v"(bv[1]), "+v"(bv[2]), "+v"(ep));
      const v4u xl = xf[3];
      if constexpr (s < 8) {
#pragma unroll
        for (int ks = 0; ks < 4; ++ks) xf[ks] = *(LAS v4u*)(xa[ks] + (s & 1) * 4096);
        readW(s, 0, wf[0]);
      }
      SCHED();
      static_for<0, 3>([&](auto fc) {
        constexpr int ft = decltype(fc)::value;
        if constexpr (s > 0) acc[ft] = mfma32(wf[1][ft], xl, acc[ft]);
        SCHED();
        slot(fc);
      });
      if constexpr (s < 8) {
        wait_lgkm0();
        static_for<0, 3>([&](auto kc) {
          constexpr int ks = decltype(kc)::value;
          readW(s, ks + 1, wf[(ks + 1) & 1]);
          SCHED();
          static_for<0, 3>([&](auto fc) {
            constexpr int ft = decltype(fc)::value;
            acc[ft] = mfma32(wf[ks & 1][ft], xf[ks], acc[ft]);
            SCHED();
            slot(std::integral_constant<int, 3 + 3 * ks + ft>{});
          });
        });
        if constexpr (s == 3) {
#pragma unroll
          for (int ft = 0; ft < 3; ++ft) acc[ft] = mfma32(v4u{bv[ft], 0u, 0u, 0u}, ones, acc[ft]);
        }
      }
    });
#pragma unroll
    for (int ft = 0; ft < 3; ++ft) {
#pragma unroll
      for (int s = 0; s < 2; ++s) {
        v4u o;
#pragma unroll
        for (int e = 0; e < 4; ++e) {
          const float a = relu(acc[ft][8 * s + 2 * e]), b = relu(acc[ft][8 * s + 2 * e + 1]);
          o[e] = pkbf(a * a, b * b);
        }
        const u32 so = (ft == 0 || j < 14) ? (u32)(((4 * i128 + w) * 86 + 6 * j + 2 * ft + s) * 1024) : H_BYTES;
        st128_sc1(o, rsH, lane * 16, so);
        SCHED();
      }
    }
  } else {
    ep = ld_flag(p.ws, WS_EPOCH);
    const v4u gwv = *(const GAS v4u*)(p.gw + lane * 8);
    LAS float* gp = (LAS float*)(sm + w * 8704);
#pragma unroll 1
    for (int b = 0; b < 4; ++b) {
      v4u xv[8];
#pragma unroll
      for (int i = 0; i < 8; ++i) xv[i] = __builtin_amdgcn_raw_buffer_load_b128(rsX, lane * 16, (i128 * 128 + 32 * w + 8 * b + i) * 1024, 0);
#pragma unroll
      for (int i = 0; i < 8; ++i) {
        float s = 0.f;
#pragma unroll
        for (int e = 0; e < 4; ++e) s = dot2bf(xv[i][e], gwv[e], s);
        gp[(8 * b + i) * 68 + lane] = s;
      }
    }
    float mine = 0.f;
#pragma unroll
    for (int i = 0; i < 8; ++i) { const f32x4 v = *(LAS f32x4*)(gp + l31 * 68 + 32 * hh + 4 * i); mine += (v[0] + v[1]) + (v[2] + v[3]); }
    mine += __shfl_xor(mine, 32);
    raw_barrier();
    const float gg = rbf(1.f / (1.f + __expf(-rbf(mine))));
    asm volatile("" : "+v"(ep));
#pragma unroll
    for (int n = 0; n < 20; ++n) pieceW2(n, n);
    if (lane < 32) __builtin_amdgcn_raw_buffer_store_b32(pkbf(gg, 1.f - gg), mkrs(p.ws + WS_G, L * 4), lane * 4, (i128 * 128 + 32 * w) * 4, SC1);
    wait_vm<0>();
  }
  const u64 tag = (((u64)(u32)__builtin_amdgcn_readfirstlane((u32)(ep >> 32)) << 32) | (u64)(u32)__builtin_amdgcn_readfirstlane((u32)ep)) + 1;
  asm volatile("" ::: "memory");

  const int q4 = (lane & 15) >> 2, p4 = lane & 3, blk = (lane >> 4) & 1;
  const int wlane = (4 * hh + q4) * 128 + (((2 * blk + (p4 >> 1)) ^ (4 * (q4 >> 1))) * 16) + 8 * (p4 & 1);
  LAS unsigned char* const wbx[2] = {sm + w * 8192 + wlane, sm + w * 8192 + (wlane ^ 64)};
  LAS unsigned char* const wbr[2] = {sm + G1_W + w * 3072 + wlane, sm + G1_W + w * 3072 + (wlane ^ 64)};
  auto rdpiece = [&](int place, int ct) { return trread(w2_stride(place) == 8192 ? wbx[ct] + w2_off(place) : wbr[ct] + (w2_off(place) - G1_W)); };
  auto readW2 = [&](int slot, v4u (&dst)[2]) {
    dst[0] = cat(rdpiece(2 * slot, 0), rdpiece(2 * slot + 1, 0));
    dst[1] = cat(rdpiece(2 * slot, 1), rdpiece(2 * slot + 1, 1));
  };
  STAMP(9);
  v4u wr[12][2];
  wait_vm<6>();
#pragma unroll
  for (int n = 0; n < 10; ++n) readW2(n, wr[n]);
  STAMP(3);
  wait_vm<0>();
  raw_barrier();
  if (threadIdx.x == 0) st_flag(p.ws, WS_HF + (i128 * 16 + j) * 8, tag);
  STAMP(4);
#pragma unroll
  for (int n = 0; n < 20; ++n) pieceW2(20 + n, n);
  STAMP(10);
  const u32 b2v = __builtin_amdgcn_raw_buffer_load_b16(mkrs(p.b2, D * 2), (c64 * 64 + 32 * (w & 1) + l31) * 2, 0, 0);
  wait_flags(p.ws, WS_HF + ((t64 >> 1) * 16 + (lane & 15)) * 8, tag);
  wait_vm<0>();
  STAMP(5);
  constexpr int WH = 6;
  v4u hf[WH][2];
  auto loadH = [&](int n, v4u (&dst)[2]) {
    const int ks = 22 * w + n;
    const u32 so = (n < 20 || w < 3) ? (u32)((2 * t64 * 86 + ks) * 1024) : H_BYTES;
    dst[0] = __builtin_amdgcn_raw_buffer_load_b128(rsH, lane * 16, so, SC1);
    dst[1] = __builtin_amdgcn_raw_buffer_load_b128(rsH, lane * 16 + 86 * 1024, so, SC1);
  };
  readW2(0, wr[10]); readW2(1, wr[11]);
  loadH(0, hf[0]); loadH(1, hf[1]);
  wait_lgkm0();
#pragma unroll
  for (int n = 0; n < 4; ++n) pieceW2(40 + n, n);
#pragma unroll
  for (int n = 2; n < WH; ++n) loadH(n, hf[n]);
  const u32 gv = __builtin_amdgcn_raw_buffer_load_b32(mkrs(p.ws + WS_G, L * 4), (t64 * 64 + 32 * (w >> 1) + l31) * 4, 0, SC1);
  STAMP(11);
  f32x16 acc[2][2] = {};
  v4u wl[2][2], a0, a1;
  u64 afl;
  bool a_ok = false;
  const rsrc_t rsA = mkrs(p.ws + WS_AT, D * L * 2);
  const u32 ao = (u32)((c64 * 64 + 32 * (w & 1) + (lane >> 2)) * (L * 2) + (t64 * 64 + 32 * (w >> 1)) * 2 + (lane & 3) * 16);
  SCHED();
  static_for<0, 22>([&](auto ic) {
    constexpr int n = decltype(ic)::value;
    if constexpr (n == 1) STAMP(12);
    if constexpr (n == 11) STAMP(13);
    if constexpr (n >= 11 && n < 21) { readW2((n + 1) % 10, wl[(n + 1) & 1]); SCHED(); }
    const v4u h0 = hf[n % WH][0], h1 = hf[n % WH][1];
    const v4u w0 = n < 12 ? wr[n < 12 ? n : 0][0] : wl[n & 1][0], w1 = n < 12 ? wr[n < 12 ? n : 0][1] : wl[n & 1][1];
    acc[0][0] = mfma32(w0, h0, acc[0][0]);
    acc[1][0] = mfma32(w1, h0, acc[1][0]);
    acc[0][1] = mfma32(w0, h1, acc[0][1]);
    acc[1][1] = mfma32(w1, h1, acc[1][1]);
    if constexpr (n + WH < 22) loadH(n + WH, hf[n % WH]);
    if constexpr (n == A_CHECK - WH) afl = ld_flag(p.ws, WS_AF + (c64 * 32 + l31) * 8);
    if constexpr (n == A_CHECK) {
      a_ok = __all(afl == tag);
      asm volatile("" ::"s"((int)a_ok) : "memory");
      a0 = __builtin_amdgcn_raw_buffer_load_b128(rsA, ao, 0, SC1);
      a1 = __builtin_amdgcn_raw_buffer_load_b128(rsA, ao, 16 * L * 2, SC1);
    }
    SCHED();
  });
  STAMP(6);
  raw_barrier();
  f32x16 own;
  {
    LAS f32x4* red = (LAS f32x4*)(sm + w * 20480) + lane;
    static_for<0, 4>([&](auto tc) {
      constexpr int T = decltype(tc)::value;
      const f32x16 v = acc[T & 1][T >> 1];
      if (T == w) own = v;
      else {
#pragma unroll
        for (int i = 0; i < 4; ++i) red[(T * 4 + i) * 64] = f32x4{v[4 * i], v[4 * i + 1], v[4 * i + 2], v[4 * i + 3]};
      }
    });
  }
  if (!a_ok) {
    wait_flags(p.ws, WS_AF + (c64 * 32 + l31) * 8, tag);
    a0 = __builtin_amdgcn_raw_buffer_load_b128(rsA, ao, 0, SC1);
    a1 = __builtin_amdgcn_raw_buffer_load_b128(rsA, ao, 16 * L * 2, SC1);
  }
  STAMP(7);
  {
    own = mfma32(v4u{b2v, 0u, 0u, 0u}, ones, own);
    raw_barrier();
#pragma unroll
    for (int o = 1; o < 4; ++o) {
      const int src = (w + o) & 3;
      LAS f32x4* red = (LAS f32x4*)(sm + src * 20480) + lane + w * 256;
#pragma unroll
      for (int i = 0; i < 4; ++i) { const f32x4 v = red[i * 64]; own[4 * i] += v[0]; own[4 * i + 1] += v[1]; own[4 * i + 2] += v[2]; own[4 * i + 3] += v[3]; }
    }
    LAS unsigned char* at = sm + w * 20480 + 16384;
    *(LAS v4u*)(at + lane * 16) = a0;
    *(LAS v4u*)(at + 1024 + lane * 16) = a1;
    const float gg = bf2f(gv & 0xffffu), og = bfhi(gv);
    v2u pk[4];
#pragma unroll
    for (int i = 0; i < 4; ++i) {
      const v2u av = trread(at + (8 * i + 4 * hh + q4) * 64 + 32 * blk + 8 * p4);
      pk[i].x = pkbf(gg * bf2f(av.x & 0xffffu) + og * own[4 * i], gg * bfhi(av.x) + og * own[4 * i + 1]);
      pk[i].y = pkbf(gg * bf2f(av.y & 0xffffu) + og * own[4 * i + 2], gg * bfhi(av.y) + og * own[4 * i + 3]);
    }
    u16* orow = p.out + (size_t)(t64 * 64 + 32 * (w >> 1) + l31) * D + c64 * 64 + 32 * (w & 1) + 8 * hh;
#pragma unroll
    for (int i = 0; i < 2; ++i) {
      const auto sx = __builtin_amdgcn_permlane32_swap(pk[2 * i].x, pk[2 * i + 1].x, false, false);
      const auto sy = __builtin_amdgcn_permlane32_swap(pk[2 * i].y, pk[2 * i + 1].y, false, false);
      *(GAS v4u*)(orow + 16 * i) = v4u{sx[0], sy[0], sx[1], sy[1]};
    }
  }
  STAMP(8);
  STAMP_OUT();
}

typedef v2f cf;
__device__ __forceinline__ cf cmul(cf a, cf b) {
  cf t, o;
  asm("v_pk_mul_f32 %0, %1, %2 op_sel_hi:[0,1]" : "=v"(t) : "v"(a), "v"(b));
  asm("v_pk_fma_f32 %0, %1, %2, %3 op_sel:[1,1,0] op_sel_hi:[1,0,1] neg_lo:[1,0,0]" : "=v"(o) : "v"(a), "v"(b), "v"(t));
  return o;
}
__device__ __forceinline__ cf cpow(float zr2, float zrev, float e) {
  const float m = __builtin_amdgcn_exp2f(zr2 * e), f = __builtin_amdgcn_fractf(zrev * e);
  return cf{m * __builtin_amdgcn_cosf(f), m * __builtin_amdgcn_sinf(f)};
}
constexpr int pair_at(int t) {
  int c = 0;
  for (int x = 0; x < 8; ++x)
    for (int y = 0; y <= 2 * x; ++y) {
      const int m = y < x ? x : (y == 2 * x ? x : y - x), n = y < x ? y : x;
      if (m + n < 8) { if (c == t) return m * 8 + n; ++c; }
    }
  return -1;
}
constexpr int pair_m(int t) { return pair_at(t) >> 3; }
constexpr int pair_n(int t) { return pair_at(t) & 7; }
static_assert(pair_at(35) >= 0 && pair_at(36) < 0, "36 products per class");
constexpr int KG_PITCH = 272, KG_WAVE = 53 * KG_PITCH;
constexpr int C_NAT = 4 * KG_WAVE, C_TR = C_NAT + 8192;
constexpr int UC_PITCH = 4160, UC_CH = 4 * UC_PITCH;
constexpr int C_RED = 2 * UC_CH;
constexpr int C_KREV = C_RED + 32768, KREV_CH = 2288 * 2;
static_assert(C_TR + 4 * 16 * 144 <= LDS_BYTES && C_KREV + 2 * KREV_CH <= LDS_BYTES && C_KREV >= 4 * KG_WAVE, "lds map");

__device__ __forceinline__ void conv_role(const Params& p, LAS unsigned char* sm, const int g, const int r) {
  STAMP_DECL;
  STAMP(0);
  const int lane = threadIdx.x & 63, w = __builtin_amdgcn_readfirstlane(threadIdx.x >> 6);
  const int hh = lane >> 5, l31 = lane & 31;
  const int cb = g * 32 + r, d = 2 * cb + (w >> 1), half = w & 1;
  __builtin_amdgcn_s_setprio(2);
  const int pt = 8 * (g >> 1) + (r & 7), pc = 4 * (g & 1) + (r >> 3);
  const GAS u16* usrc = (const GAS u16*)p.u + (size_t)(pt * 64 + 16 * w + (lane >> 3)) * D + pc * 64 + (lane & 7) * 8;
  const v4u n0 = *(const GAS v4u*)usrc, n1 = *(const GAS v4u*)(usrc + 8 * D);
  const int mi = d * NM + 64 * half + lane;
  const u32 are = p.Are[mi], aim = p.Aim[mi], bre = p.Bre[mi], bim = p.Bim[mi], cre = p.Cre[mi], cim = p.Cim[mi];
  const u32 ldt = p.logdt[d], dsk = p.Dskip[d];
  const u64 ep = ld_flag(p.ws, WS_EPOCH);
  *(LAS v4u*)(sm + C_NAT + (16 * w) * 128 + lane * 16) = n0;
  *(LAS v4u*)(sm + C_NAT + (16 * w + 8) * 128 + lane * 16) = n1;
  __syncthreads();
  STAMP(1);
  {
    LAS unsigned char* tr = sm + C_TR + w * (16 * 144);
#pragma unroll
    for (int i = 0; i < 4; ++i) {
      const v2u v = trread(sm + C_NAT + (16 * i + 4 * (lane >> 4) + ((lane & 15) >> 2)) * 128 + 32 * w + 8 * (lane & 3));
      *(LAS v2u*)(tr + (lane & 15) * 144 + (16 * i + 4 * (lane >> 4)) * 2) = v;
    }
    const rsrc_t rsUT = mkrs(p.ws + WS_UT, D * L * 2);
#pragma unroll
    for (int i = 0; i < 2; ++i) {
      const v4u v = *(LAS v4u*)(tr + (8 * i + (lane >> 3)) * 144 + (lane & 7) * 16);
      st128_sc1(v, rsUT, (lane >> 3) * (L * 2) + (lane & 7) * 16, (pc * 64 + 16 * w + 8 * i) * (L * 2) + pt * 128);
    }
  }
  __builtin_amdgcn_s_setprio(0);
  {
    const float dt = rbf(expf(bf2f(ldt)));
    const float zr2 = (dt * bf2f(are)) * 1.4426950408889634f, zrev = (dt * bf2f(aim)) * 0.15915494309189535f;
    const cf CB = cmul(cf{bf2f(cre), bf2f(cim)}, cf{dt * bf2f(bre), dt * bf2f(bim)});
    const cf w1 = cpow(zr2, zrev, 1.f), w8 = cpow(zr2, zrev, 8.f), w32 = cpow(zr2, zrev, 32.f), w256 = cpow(zr2, zrev, 256.f);
    LAS u32* tb = (LAS u32*)(sm + w * KG_WAVE) + lane;
    auto putE = [&](int b, cf v) { tb[b * (KG_PITCH / 4)] = pkbf(v.x, v.y); };
    auto putG = [&](int row, cf v) { tb[(32 + row) * (KG_PITCH / 4)] = pkbf(v.x, -v.y); };
    cf e[4];
    e[0] = cf{1.f, 0.f}; e[1] = w8; e[2] = cmul(w8, w8); e[3] = cmul(e[2], w8);
#pragma unroll
    for (int b = 0; b < 4; ++b) putE(b, e[b]);
    e[0] = w32; putE(4, e[0]);
#pragma unroll
    for (int b = 5; b < 32; ++b) { e[b & 3] = cmul(e[b & 3], w32); putE(b, e[b & 3]); }
    cf G[8];
    G[0] = CB;
#pragma unroll
    for (int f = 1; f < 8; ++f) G[f] = cmul(G[f - 1], w1);
#pragma unroll
    for (int f = 0; f < 8; ++f) putG(f, G[f]);
    cf a0 = cmul(G[0], w256), a4 = cmul(G[4], w256);
    putG(8, a0); putG(9, cmul(G[2], w256)); putG(10, a4); putG(11, cmul(G[6], w256));
    a0 = cmul(a0, w256); a4 = cmul(a4, w256); putG(12, a0); putG(13, a4);
    a0 = cmul(a0, w256); a4 = cmul(a4, w256); putG(14, a0); putG(15, a4);
#pragma unroll
    for (int f = 16; f < 21; ++f) { a0 = cmul(a0, w256); putG(f, a0); }
    LAS unsigned char* pa = sm + w * KG_WAVE + (32 + min(l31, 20)) * KG_PITCH + 16 * hh;
    LAS unsigned char* pb = sm + w * KG_WAVE + l31 * KG_PITCH + 16 * hh;
    f32x16 kd = {};
#pragma unroll
    for (int ks = 0; ks < 8; ++ks) kd = mfma32(*(LAS v4u*)(pa + 32 * ks), *(LAS v4u*)(pb + 32 * ks), kd);
    LAS float* T = (LAS float*)(sm + w * KG_WAVE);
    *(LAS f32x4*)(T + 8 * l31 + 4 * hh) = f32x4{kd[0], kd[1], kd[2], kd[3]};
    *(LAS v2f*)(T + (hh ? 384 + 2 * l31 : 256 + 4 * l31)) = v2f{kd[4], kd[5]};
    *(LAS v2f*)(T + (hh ? 448 + 2 * l31 : 258 + 4 * l31)) = v2f{kd[6], kd[7]};
#pragma unroll
    for (int i = 0; i < 4; ++i) T[512 + 128 * hh + 32 * i + l31] = kd[8 + i];
  }
  const u64 tag = ep + 1;
  __builtin_amdgcn_s_setprio(2);
  wait_vm<0>();
  __syncthreads();
  if (threadIdx.x == 0) st_flag(p.ws, WS_PF + (pc * 32 + pt) * 8, tag);
  STAMP(2);
  {
    const LAS float* T0 = (const LAS float*)(sm + (w & 2) * KG_WAVE);
    const LAS float* T1 = (const LAS float*)(sm + ((w & 2) + 1) * KG_WAVE);
    LAS v4u* kr = (LAS v4u*)(sm + C_KREV + (w >> 1) * KREV_CH);
    float v[8];
    int c;
    if (half == 0) {
      c = lane;
      const int base = lane < 32 ? 8 * lane : 256 + 4 * (lane - 32);
      const f32x4 x0 = *(const LAS f32x4*)(T0 + base) + *(const LAS f32x4*)(T1 + base), x1 = *(const LAS f32x4*)(T0 + base + 4) + *(const LAS f32x4*)(T1 + base + 4);
      const bool s0 = lane < 32;
      v[0] = x0[0]; v[1] = s0 ? x0[1] : x0[0]; v[2] = s0 ? x0[2] : x0[1]; v[3] = s0 ? x0[3] : x0[2];
      v[4] = s0 ? x1[0] : x0[2]; v[5] = s0 ? x1[1] : x0[2]; v[6] = s0 ? x1[2] : x0[3]; v[7] = s0 ? x1[3] : x1[0];
    } else {
      c = 64 + lane;
      const int base = 384 + 2 * lane;
      const v2f x0 = *(const LAS v2f*)(T0 + base) + *(const LAS v2f*)(T1 + base);
      const float x2 = T0[base + 2] + T1[base + 2];
      v[0] = v[1] = v[2] = x0[0]; v[3] = v[4] = v[5] = x0[1]; v[6] = v[7] = x2;
    }
    kr[255 - c] = v4u{pkbf(v[7], v[6]), pkbf(v[5], v[4]), pkbf(v[3], v[2]), pkbf(v[1], v[0])};
    {
      const int c3 = 128 + 64 * half + lane, base = 512 + 64 * half + lane;
      const float y0 = T0[base] + T1[base], y1 = T0[base + 1] + T1[base + 1];
      const u32 lo = pkbf(y0, y0), hi = pkbf(y1, y1);
      kr[255 - c3] = v4u{hi, (lane & 1) ? hi : pkbf(y1, y0), lo, lo};
    }
    if (half == 1 && lane < 30) kr[256 + lane] = v4u{0u, 0u, 0u, 0u};
  }
  wait_flags(p.ws, WS_PF + (g * 32 + l31) * 8, tag);
  STAMP(3);
  {
    const rsrc_t rsU = mkrs(p.ws + WS_UT + (size_t)d * (L * 2), L * 2);
    v4u E[2]; v2u X[2];
#pragma unroll
    for (int i = 0; i < 2; ++i) {
      const int n = 128 * half + 64 * i + lane;
      E[i] = __builtin_amdgcn_raw_buffer_load_b128(rsU, n * 16, 0, SC1);
      X[i] = __builtin_amdgcn_raw_buffer_load_b64(rsU, n * 16 + 16, 0, SC1);
    }
    __syncthreads();
    LAS v4u* uc = (LAS v4u*)(sm + (w >> 1) * UC_CH);
    auto put = [&](int c, v4u e, v2u x) {
      const u32 o0 = __builtin_amdgcn_alignbit(e.y, e.x, 16), o1 = __builtin_amdgcn_alignbit(e.z, e.y, 16), o2 = __builtin_amdgcn_alignbit(e.w, e.z, 16),
                o3 = __builtin_amdgcn_alignbit(x.x, e.w, 16), o4 = __builtin_amdgcn_alignbit(x.y, x.x, 16);
      uc[c] = e;
      uc[UC_PITCH / 16 + c] = v4u{o0, o1, o2, o3};
      uc[2 * (UC_PITCH / 16) + c] = v4u{e.y, e.z, e.w, x.x};
      uc[3 * (UC_PITCH / 16) + c] = v4u{o1, o2, o3, o4};
    };
#pragma unroll
    for (int i = 0; i < 2; ++i) put(128 * half + 64 * i + lane + 2, E[i], X[i]);
    if (half == 0) {
      const u32 f0 = __builtin_amdgcn_readfirstlane(E[0].x), f1 = __builtin_amdgcn_readfirstlane(E[0].y);
      if (lane < 2) put(lane, v4u{0u, 0u, 0u, 0u}, lane ? v2u{f0, f1} : v2u{0u, 0u});
    }
  }
  __syncthreads();
  STAMP(4);
  __builtin_amdgcn_s_setprio(0);
  f32x4 acc[8] = {};
  {
    const int r16 = lane & 15, q = lane >> 4;
    LAS unsigned char* ka = sm + C_KREV + (w >> 1) * KREV_CH + 4512 - 3584 - 32 * r16 + 16 * q - 256 * half;
    LAS unsigned char* ua = sm + (w >> 1) * UC_CH + ((r16 + 1) & 3) * UC_PITCH + 8 * ((r16 + 1) >> 2) + 16 * q + 64 * 7 - 256 * half;
    ka -= 192; ua -= 192;
    LAS unsigned char* ub = ua + 8;
    asm volatile("" : "+v"(ub));
    v2u ulo[2][8], uhi[2][8];
    v4u kf[2][8];
    auto fetch = [&](int rho, int i) {
      const int n = i / 3, k = i % 3, o = 64 * (3 - rho);
      if (k == 0) kf[rho & 1][n] = *(LAS v4u*)(ka + o + 512 * (7 - n));
      else if (k == 1) ulo[rho & 1][n] = *(LAS v2u*)(ua + o + 512 * n);
      else uhi[rho & 1][n] = *(LAS v2u*)(ub + o + 512 * n);
    };
#pragma unroll
    for (int i = 0; i < 24; ++i) fetch(0, i);
    SCHED();
    static_for<0, 4>([&](auto rc) {
      constexpr int rho = decltype(rc)::value;
      static_for<0, 36>([&](auto tc) {
        constexpr int t = decltype(tc)::value;
        constexpr int mm = pair_m(t), nn = pair_n(t);
        acc[mm + nn] = mfma16(cat(ulo[rho & 1][nn], uhi[rho & 1][nn]), kf[rho & 1][mm], acc[mm + nn]);
        if constexpr (rho < 3 && t % 3 != 2) fetch(rho + 1, 2 * (t / 3) + t % 3);
        if constexpr (t % 3 == 2) SCHED();
      });
    });
    LAS unsigned char* rd = sm + C_RED + w * 8192 + 64 * r16 + 16 * (q ^ ((r16 >> 1) & 3));
#pragma unroll
    for (int i = 0; i < 8; ++i) *(LAS f32x4*)(rd + 1024 * i) = acc[i];
  }
  __syncthreads();
  STAMP(5);
  __builtin_amdgcn_s_setprio(2);
  {
    const float ds = bf2f(dsk);
    const rsrc_t rsA = mkrs(p.ws + WS_AT, D * L * 2);
#pragma unroll
    for (int i = 0; i < 2; ++i) {
      const int n = 128 * half + 64 * i + lane, row = n >> 1, sw = (row >> 1) & 3, hq = n & 1;
      const LAS unsigned char* b0 = sm + C_RED + (w & 2) * 8192 + 64 * row;
      const f32x4 y0 = *(const LAS f32x4*)(b0 + 16 * ((2 * hq) ^ sw)) + *(const LAS f32x4*)(b0 + 8192 + 16 * ((2 * hq) ^ sw));
      const f32x4 y1 = *(const LAS f32x4*)(b0 + 16 * ((2 * hq + 1) ^ sw)) + *(const LAS f32x4*)(b0 + 8192 + 16 * ((2 * hq + 1) ^ sw));
      const v4u uu = *(const LAS v4u*)(sm + (w >> 1) * UC_CH + (n + 2) * 16);
      v4u o;
#pragma unroll
      for (int e = 0; e < 4; ++e) {
        const float ya = e < 2 ? y0[2 * e] : y1[2 * e - 4], yb = e < 2 ? y0[2 * e + 1] : y1[2 * e - 3];
        const u32 yy = pkbf(ya, yb), ud = pkbf(bf2f(uu[e] & 0xffffu) * ds, bfhi(uu[e]) * ds);
        o[e] = pkbf(bf2f(yy & 0xffffu) + bf2f(ud & 0xffffu), bfhi(yy) + bfhi(ud));
      }
      st128_sc1(o, rsA, n * 16, d * (L * 2));
    }
  }
  wait_vm<0>();
  __syncthreads();
  if (threadIdx.x == 0) st_flag(p.ws, WS_AF + cb * 8, tag);
  STAMP(6);
  if (cb == 0 && w == 0) {
#pragma unroll
    for (int i = 0; i < 4; ++i) {
      wait_flags(p.ws, WS_HF + (64 * i + lane) * 8, tag);
      wait_flags(p.ws, WS_AF + (64 * i + lane) * 8, tag); }
    if (lane == 0) st_flag(p.ws, WS_EPOCH, tag);
  }
  STAMP_OUT();
}

__global__ __launch_bounds__(256, 2) void gated_mix_kernel(Params p) {
  __shared__ __attribute__((aligned(16))) unsigned char smem[LDS_BYTES];
  const int g = blockIdx.x & 7, loc = blockIdx.x >> 3;
  if (loc >= 32) {
    __builtin_amdgcn_s_setprio(1);
    ffn_role(p, (LAS unsigned char*)smem, g, loc - 32);
  } else {
    conv_role(p, (LAS unsigned char*)smem, g, loc);
  }
}

extern "C" void kernel_launch(void* const* d_in, const int* in_sizes, int n_in, void* d_out, int out_size, void* d_ws, size_t ws_size,
                              hipStream_t stream) {
  Params p;
  p.u = (const u16*)d_in[0]; p.Are = (const u16*)d_in[1]; p.Aim = (const u16*)d_in[2]; p.Bre = (const u16*)d_in[3];
  p.Bim = (const u16*)d_in[4]; p.Cre = (const u16*)d_in[5]; p.Cim = (const u16*)d_in[6]; p.logdt = (const u16*)d_in[7];
  p.Dskip = (const u16*)d_in[8]; p.w1 = (const u16*)d_in[10]; p.b1 = (const u16*)d_in[11]; p.w2 = (const u16*)d_in[12];
  p.b2 = (const u16*)d_in[13]; p.gw = (const u16*)d_in[14]; p.out = (u16*)d_out; p.ws = (unsigned char*)d_ws;
  gated_mix_kernel<<<512, 256, 0, stream>>>(p);
}
```

```cpp
#include <hip/hip_runtime.h>
#include <stdint.h>

typedef unsigned short u16;
typedef unsigned u32;
#define HROT 2
constexpr u32 WS_G = 32768, WS_UT = 1u << 20, WS_AT = 3u << 20, WS_H = 5u << 20, WS_STAMP = 200u << 20;

struct Params {
  const u16 *u, *Are, *Aim, *Bre, *Bim, *Cre, *Cim, *logdt, *Dskip, *w1, *b1, *w2, *b2, *gw;
  u16* out;
  unsigned char* ws;
};
static_assert(sizeof(Params) == 128, "argument offsets are built into the code below");

__global__ __launch_bounds__(512, 2) void gated_mix_kernel(Params p) {
  __shared__ __attribute__((aligned(16))) unsigned char smem[163840];
  asm volatile("" ::"v"((__attribute__((address_space(3))) unsigned char*)smem));
  const auto ka = __builtin_amdgcn_kernarg_segment_ptr();
  asm volatile(
      "v_readfirstlane_b32 s23, %[tid]\n\t"
      "s_getpc_b64 s[20:21]\n\t"
      "998:\n\t"
      "s_cmpk_lg_u32 s23, 256\n\t"
      "s_cbranch_scc1 997f\n\t"
      "s_lshr_b32 s22, %[bid], 3\n\t"
      "s_mul_i32 s22, s22, 896\n\t"
      "v_and_b32 v254, 63, %[tid]\n\t"
      "v_lshlrev_b32 v254, 7, v254\n\t"
      "v_add_u32 v254, s22, v254\n\t"
      "s_mov_b32 s22, 999f-998b-4\n\t"
      "v_min_u32 v254, s22, v254\n\t"
      "s_mov_b64 exec, 0x7f\n\t"
      "s_nop 0\n\t"
      "global_load_dword v255, v254, s[20:21]\n\t"
      "s_mov_b64 exec, -1\n\t"
      "997:\n\t"
      "s_cmpk_lt_u32 s23, 0x100\n\t"
      "s_cbranch_scc0 200f\n\t"
      "s_load_dwordx16 s[24:39], %[ka], 0x8\n\t"
      "s_load_dwordx2 s[40:41], %[ka], 0x78\n\t"
      "s_load_dwordx2 s[42:43], %[ka], 0x58\n\t"
      "s_setprio 2\n\t"
      "v_bfe_u32 v19, %[tid], 6, 2\n\t"
      "v_mov_b32 v24, 0\n\t"
      "v_lshlrev_b32 v19, 2, v19\n\t"
      "v_add_u32 v19, 81856, v19\n\t"
      "ds_write_b32 v19, v24\n\t"
      "v_readfirstlane_b32 s56, %[tid]\n\t"
      "v_and_b32 v1, 63, %[tid]\n\t"
      "v_and_b32 v2, 31, %[tid]\n\t"
      "v_bfe_u32 v3, %[tid], 5, 1\n\t"
      "s_bfe_u32 s56, s56, 0x20006\n\t"
      "s_and_b32 s57, s56, 1\n\t"
      "s_lshr_b32 s58, s56, 1\n\t"
      "s_and_b32 s61, %[bid], 7\n\t"
      "s_lshl_b32 s61, s61, 5\n\t"
      "s_lshr_b32 s62, %[bid], 3\n\t"
      "s_add_u32 s59, s61, s62\n\t"
      "s_lshl1_add_u32 s60, s59, s58\n\t"
      "s_lshl1_add_u32 s61, s60, s57\n\t"
      "s_lshl_b32 s61, s61, 7\n\t"
      "v_lshlrev_b32 v22, 1, v1\n\t"
      "v_add_u32 v22, s61, v22\n\t"
      "s_lshl_b32 s61, s60, 1\n\t"
      "v_mov_b32 v23, s61\n\t"
      "v_mov_b32 v24, 0\n\t"
      "s_waitcnt lgkmcnt(0)\n\t"
      "global_load_dwordx2 v[10:11], v24, s[40:41] sc1\n\t"
      "global_load_ushort v36, v22, s[24:25]\n\t"
      "global_load_ushort v37, v22, s[26:27]\n\t"
      "global_load_ushort v38, v22, s[28:29]\n\t"
      "global_load_ushort v39, v22, s[30:31]\n\t"
      "global_load_ushort v40, v22, s[32:33]\n\t"
      "global_load_ushort v41, v22, s[34:35]\n\t"
      "global_load_ushort v42, v23, s[36:37]\n\t"
      "global_load_ushort v8, v23, s[38:39]\n\t"
      "s_barrier\n\t"
      "s_branch 201f\n\t"
      "200:\n\t"
      "s_setprio 1\n\t"
      "s_load_dwordx2 s[24:25], %[ka], 0x0\n\t"
      "s_load_dwordx8 s[40:47], %[ka], 0x48\n\t"
      "s_load_dwordx4 s[76:79], %[ka], 0x68\n\t"
      "s_load_dwordx2 s[72:73], %[ka], 0x78\n\t"
      "v_readfirstlane_b32 s84, %[tid]\n\t"
      "v_and_b32 v1, 63, %[tid]\n\t"
      "v_and_b32 v2, 31, %[tid]\n\t"
      "v_bfe_u32 v3, %[tid], 5, 1\n\t"
      "s_bfe_u32 s84, s84, 0x20006\n\t"
      "s_bfe_u32 s57, %[bid], 0x20001\n\t"
      "s_and_b32 s62, %[bid], 1\n\t"
      "s_lshr_b32 s63, %[bid], 6\n\t"
      "s_lshl2_add_u32 s80, s57, s63\n\t"
      "s_lshl2_add_u32 s95, s62, s63\n\t"
      "s_bfe_u32 s63, %[bid], 0x30003\n\t"
      "s_lshl3_add_u32 s81, s62, s63\n\t"
      "s_lshl3_add_u32 s85, s57, s63\n\t"
      "s_lshr_b32 s63, %[bid], 5\n\t"
      "s_lshl3_add_u32 s82, s57, s63\n\t"
      "s_bfe_u32 s63, %[bid], 0x20003\n\t"
      "s_lshl2_add_u32 s83, s62, s63\n\t"
      "s_and_b32 s63, s81, 1\n\t"
      "s_lshl1_add_u32 s99, s63, s57\n\t"
      "s_add_u32 s99, s99, s62\n\t"
      "s_and_b32 s63, s80, 1\n\t"
      "s_lshl1_add_u32 s99, s99, s63\n\t"
      "s_lshl_b32 s60, s84, 13\n\t"
      "s_add_u32 s60, s60, 81920\n\t"
      "s_mul_i32 s61, s84, 3072\n\t"
      "s_add_u32 s61, s61, 114688\n\t"
      "v_lshrrev_b32 v28, 3, v1\n\t"
      "v_and_b32 v29, 7, v1\n\t"
      "v_lshlrev_b32 v17, 10, v28\n\t"
      "v_lshl_add_u32 v17, v29, 4, v17\n\t"
      "s_lshl2_add_u32 s57, s85, s84\n\t"
      "s_lshl_b32 s57, s57, 14\n\t"
      "s_lshl_b32 s62, s95, 7\n\t"
      "s_add_u32 s91, s57, s62\n\t"
      "s_add_u32 s92, s91, 8192\n\t"
      "v_lshrrev_b32 v27, 4, v1\n\t"
      "v_xor_b32 v27, v29, v27\n\t"
      "s_lshl_b32 s57, s84, 15\n\t"
      "v_lshlrev_b32 v4, 10, v28\n\t"
      "v_lshl_add_u32 v4, v27, 4, v4\n\t"
      "v_add_u32 v4, s57, v4\n\t"
      "v_xor_b32 v5, 64, v4\n\t"
      "v_add_u32 v6, 14336, v4\n\t"
      "v_add_u32 v7, 21504, v5\n\t"
      "v_add_u32 v5, 7168, v5\n\t"
      "s_mul_i32 s57, s84, 192\n\t"
      "s_movk_i32 s62, 2560\n\t"
      "v_add_u32 v27, s57, v1\n\t"
      "v_mul_u32_u24 v8, 0xaaab, v27\n\t"
      "v_lshrrev_b32 v8, 19, v8\n\t"
      "v_mul_u32_u24 v8, s62, v8\n\t"
      "v_lshl_add_u32 v8, v27, 4, v8\n\t"
      "v_add_u32 v27, s57, v1\n\t"
      "v_add_u32 v27, 64, v27\n\t"
      "v_mul_u32_u24 v9, 0xaaab, v27\n\t"
      "v_lshrrev_b32 v9, 19, v9\n\t"
      "v_mul_u32_u24 v9, s62, v9\n\t"
      "v_lshl_add_u32 v9, v27, 4, v9\n\t"
      "v_subrev_u32 v9, 1024, v9\n\t"
      "v_add_u32 v27, s57, v1\n\t"
      "v_add_u32 v27, 128, v27\n\t"
      "v_mul_u32_u24 v10, 0xaaab, v27\n\t"
      "v_lshrrev_b32 v10, 19, v10\n\t"
      "v_mul_u32_u24 v10, s62, v10\n\t"
      "v_lshl_add_u32 v10, v27, 4, v10\n\t"
      "v_subrev_u32 v10, 2048, v10\n\t"
      "s_lshl_b32 s93, s80, 17\n\t"
      "s_mul_i32 s98, s81, 192\n\t"
      "v_mov_b32 v31, 0\n\t"
      "s_waitcnt lgkmcnt(0)\n\t"
      "s_and_b32 s25, s25, 0xffff\n\t"
      "s_mov_b32 s26, 2097152\n\t"
      "s_mov_b32 s27, 0x20000\n\t"
      "global_load_dwordx2 v[24:25], v31, s[72:73] sc1\n\t"
      "buffer_load_dwordx4 v[164:167], v17, s[24:27], s91 offen\n\t"
      "buffer_load_dwordx4 v[168:171], v17, s[24:27], s92 offen\n\t"
      "s_mov_b32 s28, s40\n\t"
      "s_and_b32 s29, s41, 0xffff\n\t"
      "s_mov_b32 s30, 1409024\n\t"
      "s_mov_b32 s31, 0x20000\n\t"
      "s_mov_b32 s36, s42\n\t"
      "s_and_b32 s37, s43, 0xffff\n\t"
      "s_mov_b32 s38, 2752\n\t"
      "s_mov_b32 s39, 0x20000\n\t"
      "s_mov_b32 s32, s44\n\t"
      "s_and_b32 s33, s45, 0xffff\n\t"
      "s_mov_b32 s34, 1409024\n\t"
      "s_mov_b32 s35, 0x20000\n\t"
      "s_mov_b64 s[74:75], s[46:47]\n\t"
      "s_cmp_eq_u32 s81, 15\n\t"
      "s_cbranch_scc1 30f\n\t"
      "s_add_u32 s57, s99, 0\n\t"
      "s_and_b32 s57, s57, 7\n\t"
      "s_lshl_b32 s40, s57, 7\n\t"
      "s_add_u32 s40, s40, s93\n\t"
      "s_mul_i32 s48, s57, 176128\n\t"
      "s_add_u32 s48, s48, s98\n\t"
      "s_add_u32 m0, s60, 0\n\t"
      "s_nop 0\n\t"
      "buffer_load_dwordx4 v4, s[24:27], s40 offen lds\n\t"
      "buffer_load_dwordx4 v5, s[24:27], s40 offen offset:1024 lds\n\t"
      "buffer_load_dwordx4 v6, s[24:27], s40 offen offset:2048 lds\n\t"
      "buffer_load_dwordx4 v7, s[24:27], s40 offen offset:3072 lds\n\t"
      "s_add_u32 m0, s61, 0\n\t"
      "s_nop 0\n\t"
      "buffer_load_dwordx4 v8, s[28:31], s48 offen lds\n\t"
      "buffer_load_dwordx4 v9, s[28:31], s48 offen offset:1024 lds\n\t"
      "buffer_load_dwordx4 v10, s[28:31], s48 offen offset:2048 lds\n\t"
      "s_add_u32 s57, s99, 1\n\t"
      "s_and_b32 s57, s57, 7\n\t"
      "s_lshl_b32 s41, s57, 7\n\t"
      "s_add_u32 s41, s41, s93\n\t"
      "s_mul_i32 s49, s57, 176128\n\t"
      "s_add_u32 s49, s49, s98\n\t"
      "s_add_u32 m0, s60, 4096\n\t"
      "s_nop 0\n\t"
      "buffer_load_dwordx4 v4, s[24:27], s41 offen lds\n\t"
      "buffer_load_dwordx4 v5, s[24:27], s41 offen offset:1024 lds\n\t"
      "s_lshl_b32 s57, s84, 2\n\t"
      "s_add_u32 s57, s57, 81888\n\t"
      "v_mov_b32 v180, 81888\n\t"
      "v_mov_b32 v181, s57\n\t"
      "v_mov_b32 v182, 0\n\t"
      "ds_write_b32 v181, v182\n\t"
      "s_waitcnt lgkmcnt(0)\n\t"
      "s_barrier\n\t"
      "buffer_load_dwordx4 v6, s[24:27], s41 offen offset:2048 lds\n\t"
      "s_lshl_b32 s57, s84, 11\n\t"
      "s_add_u32 s57, s57, 34816\n\t"
      "v_lshlrev_b32 v18, 4, v1\n\t"
      "v_add_u32 v18, s57, v18\n\t"
      "v_lshrrev_b32 v28, 4, v1\n\t"
      "v_bfe_u32 v29, v1, 2, 2\n\t"
      "v_lshl_add_u32 v19, v28, 2, v29\n\t"
      "v_and_b32 v27, 3, v1\n\t"
      "v_lshlrev_b32 v19, 7, v19\n\t"
      "v_lshl_add_u32 v19, v27, 3, v19\n\t"
      "s_lshl_b32 s57, s84, 5\n\t"
      "s_add_u32 s57, s57, 34816\n\t"
      "v_add_u32 v19, s57, v19\n\t"
      "s_mul_i32 s57, s84, 2304\n\t"
      "s_add_u32 s57, s57, 43008\n\t"
      "s_movk_i32 s62, 144\n\t"
      "v_and_b32 v20, 15, v1\n\t"
      "v_mul_u32_u24 v20, s62, v20\n\t"
      "v_lshl_add_u32 v20, v28, 3, v20\n\t"
      "v_add_u32 v20, s57, v20\n\t"
      "v_lshrrev_b32 v22, 3, v1\n\t"
      "v_and_b32 v26, 7, v1\n\t"
      "v_mul_u32_u24 v21, s62, v22\n\t"
      "v_lshl_add_u32 v21, v26, 4, v21\n\t"
      "v_add_u32 v21, s57, v21\n\t"
      "v_and_b32 v11, 1, v28\n\t"
      "v_lshlrev_b32 v11, 2, v11\n\t"
      "v_xor_b32 v11, v26, v11\n\t"
      "v_lshlrev_b32 v11, 4, v11\n\t"
      "v_lshl_add_u32 v11, v22, 10, v11\n\t"
      "v_lshlrev_b32 v22, 12, v22\n\t"
      "v_lshl_add_u32 v22, v26, 4, v22\n\t"
      "s_lshl2_add_u32 s57, s95, s84\n\t"
      "s_lshl_b32 s62, s57, 5\n\t"
      "s_add_u32 s62, s62, s85\n\t"
      "s_lshl_b32 s62, s62, 3\n\t"
      "s_add_u32 s62, s62, 8192\n\t"
      "v_mov_b32 v23, s62\n\t"
      "s_lshl_b32 s57, s57, 16\n\t"
      "s_lshl_b32 s62, s85, 7\n\t"
      "s_add_u32 s85, s57, s62\n\t"
      "s_mul_i32 s89, s84, 360448\n\t"
      "s_lshl_b32 s62, s83, 7\n\t"
      "s_add_u32 s89, s89, s62\n\t"
      "s_add_u32 s90, s89, 360448\n\t"
      "s_and_b32 s95, s82, 3\n\t"
      "s_mul_i32 s95, s95, 11\n\t"
      "s_lshr_b32 s95, s95, 1\n\t"
      "s_lshl_b32 s62, s95, 14\n\t"
      "s_add_u32 s56, s89, s62\n\t"
      "s_mul_i32 s91, s84, 12288\n\t"
      "s_add_u32 s57, s99, 2\n\t"
      "s_and_b32 s57, s57, 7\n\t"
      "s_lshl_b32 s42, s57, 7\n\t"
      "s_add_u32 s42, s42, s93\n\t"
      "s_mul_i32 s50, s57, 176128\n\t"
      "s_add_u32 s50, s50, s98\n\t"
      "v_bfe_u32 v29, v1, 2, 2\n\t"
      "v_bfe_u32 v28, v2, 1, 3\n\t"
      "v_xor_b32 v28, v3, v28\n\t"
      "v_lshlrev_b32 v12, 7, v2\n\t"
      "v_lshl_add_u32 v12, v28, 4, v12\n\t"
      "v_add_u32 v12, s60, v12\n\t"
      "v_xor_b32 v13, 32, v12\n\t"
      "v_xor_b32 v14, 64, v12\n\t"
      "v_xor_b32 v15, 96, v12\n\t"
      "v_lshl_add_u32 v16, v3, 3, v29\n\t"
      "v_mul_u32_u24 v16, 192, v16\n\t"
      "v_bfe_u32 v28, v1, 4, 1\n\t"
      "v_lshl_add_u32 v16, v28, 5, v16\n\t"
      "v_and_b32 v28, 3, v1\n\t"
      "v_lshl_add_u32 v16, v28, 3, v16\n\t"
      "v_add_u32 v16, 114688, v16\n\t"
      "s_mul_i32 s57, s81, 192\n\t"
      "v_lshlrev_b32 v26, 1, v2\n\t"
      "v_add_u32 v26, s57, v26\n\t"
      "s_waitcnt vmcnt(11)\n\t"
      "ds_write_b128 v18, v[164:167]\n\t"
      "v_mov_b32 v182, 1\n\t"
      "s_waitcnt vmcnt(10)\n\t"
      "ds_write_b128 v18, v[168:171] offset:1024\n\t"
      "ds_write_b32 v181, v182\n\t"
      "buffer_load_dwordx4 v7, s[24:27], s41 offen offset:3072 lds\n\t"
      "s_add_u32 m0, s61, 12288\n\t"
      "s_nop 0\n\t"
      "buffer_load_dwordx4 v8, s[28:31], s49 offen lds\n\t"
      "s_mov_b32 s88, 0x400000\n\t"
      "40:\n\t"
      "ds_read_b128 v[184:187], v180\n\t"
      "s_waitcnt lgkmcnt(0)\n\t"
      "v_min_u32 v184, v184, v185\n\t"
      "v_min3_u32 v184, v184, v186, v187\n\t"
      "v_cmp_gt_u32 vcc, 1, v184\n\t"
      "s_sub_u32 s88, s88, 1\n\t"
      "s_cbranch_scc1 41f\n\t"
      "s_nop 2\n\t"
      "s_cbranch_vccnz 40b\n\t"
      "41:\n\t"
      "ds_read_b64_tr_b16 v[172:173], v19 offset:0\n\t"
      "ds_read_b64_tr_b16 v[174:175], v19 offset:2048\n\t"
      "ds_read_b64_tr_b16 v[176:177], v19 offset:4096\n\t"
      "ds_read_b64_tr_b16 v[178:179], v19 offset:6144\n\t"
      "buffer_load_dwordx4 v9, s[28:31], s49 offen offset:1024 lds\n\t"
      "buffer_load_dwordx4 v10, s[28:31], s49 offen offset:2048 lds\n\t"
      "s_waitcnt lgkmcnt(3)\n\t"
      "ds_write_b64 v20, v[172:173] offset:0\n\t"
      "s_waitcnt lgkmcnt(3)\n\t"
      "ds_write_b64 v20, v[174:175] offset:32\n\t"
      "s_waitcnt lgkmcnt(3)\n\t"
      "ds_write_b64 v20, v[176:177] offset:64\n\t"
      "s_waitcnt lgkmcnt(3)\n\t"
      "ds_write_b64 v20, v[178:179] offset:96\n\t"
      "ds_read_b128 v[164:167], v21\n\t"
      "ds_read_b128 v[168:171], v21 offset:1152\n\t"
      "v_add_co_u32 v24, vcc, 1, v24\n\t"
      "s_nop 1\n\t"
      "v_addc_co_u32 v25, vcc, 0, v25, vcc\n\t"
      "v_readfirstlane_b32 s94, v24\n\t"
      "s_nop 0\n\t"
      "s_and_b32 s94, s94, 1\n\t"
      "s_lshl_b32 s94, s94, 24\n\t"
      "s_add_u32 s86, s72, s94\n\t"
      "s_addc_u32 s87, s73, 0\n\t"
      "s_add_u32 s68, s86, 1048576\n\t"
      "s_addc_u32 s69, s87, 0\n\t"
      "s_and_b32 s69, s69, 0xffff\n\t"
      "s_mov_b32 s70, 2097152\n\t"
      "s_mov_b32 s71, 0x20000\n\t"
      "s_add_u32 s63, s85, 32768\n\t"
      "s_waitcnt lgkmcnt(1)\n\t"
      "buffer_store_dwordx4 v[164:167], v22, s[68:71], s85 offen sc1\n\t"
      "s_waitcnt lgkmcnt(0)\n\t"
      "buffer_store_dwordx4 v[168:171], v22, s[68:71], s63 offen sc1\n\t"
      "s_waitcnt vmcnt(9)\n\t"
      "s_waitcnt lgkmcnt(0)\n\t"
      "v_mov_b32 v182, 2\n\t"
      "ds_write_b32 v181, v182\n\t"
      "s_mov_b32 s88, 0x400000\n\t"
      "44:\n\t"
      "ds_read_b128 v[184:187], v180\n\t"
      "s_waitcnt lgkmcnt(0)\n\t"
      "v_min_u32 v184, v184, v185\n\t"
      "v_min3_u32 v184, v184, v186, v187\n\t"
      "v_cmp_gt_u32 vcc, 2, v184\n\t"
      "s_sub_u32 s88, s88, 1\n\t"
      "s_cbranch_scc1 45f\n\t"
      "s_nop 2\n\t"
      "s_cbranch_vccnz 44b\n\t"
      "45:\n\t"
      "ds_read_b128 v[116:119], v12 offset:0\n\t"
      "ds_read_b128 v[120:123], v13 offset:0\n\t"
      "ds_read_b128 v[124:127], v14 offset:0\n\t"
      "ds_read_b128 v[128:131], v15 offset:0\n\t"
      "ds_read_b64_tr_b16 v[80:81], v16 offset:0\n\t"
      "ds_read_b64_tr_b16 v[82:83], v16 offset:768\n\t"
      "ds_read_b64_tr_b16 v[84:85], v16 offset:64\n\t"
      "ds_read_b64_tr_b16 v[86:87], v16 offset:832\n\t"
      "ds_read_b64_tr_b16 v[88:89], v16 offset:128\n\t"
      "ds_read_b64_tr_b16 v[90:91], v16 offset:896\n\t"
      "ds_read_b64_tr_b16 v[92:93], v16 offset:3072\n\t"
      "ds_read_b64_tr_b16 v[94:95], v16 offset:3840\n\t"
      "ds_read_b64_tr_b16 v[96:97], v16 offset:3136\n\t"
      "ds_read_b64_tr_b16 v[98:99], v16 offset:3904\n\t"
      "ds_read_b64_tr_b16 v[100:101], v16 offset:3200\n\t"
      "ds_read_b64_tr_b16 v[102:103], v16 offset:3968\n\t"
      "s_add_u32 m0, s61, 24576\n\t"
      "s_nop 0\n\t"
      "buffer_load_dwordx4 v8, s[28:31], s50 offen lds\n\t"
      "buffer_load_dwordx4 v9, s[28:31], s50 offen offset:1024 lds\n\t"
      "buffer_load_dwordx4 v10, s[28:31], s50 offen offset:2048 lds\n\t"
      "buffer_load_ushort v148, v26, s[36:39], 0 offen\n\t"
      "buffer_load_ushort v152, v26, s[36:39], 0 offen offset:64\n\t"
      "buffer_load_ushort v156, v26, s[36:39], 0 offen offset:128\n\t"
      "s_waitcnt lgkmcnt(6)\n\t"
      "v_mfma_f32_32x32x16_bf16 v[32:47], v[80:83], v[116:119], 0\n\t"
      "ds_read_b64_tr_b16 v[104:105], v16 offset:6144\n\t"
      "ds_read_b64_tr_b16 v[106:107], v16 offset:6912\n\t"
      "s_add_u32 m0, s60, 0\n\t"
      "s_nop 0\n\t"
      "buffer_load_dwordx4 v4, s[24:27], s42 offen lds\n\t"
      "v_mfma_f32_32x32x16_bf16 v[48:63], v[84:87], v[116:119], 0\n\t"
      "ds_read_b64_tr_b16 v[108:109], v16 offset:6208\n\t"
      "ds_read_b64_tr_b16 v[110:111], v16 offset:6976\n\t"
      "v_mov_b32 v149, 0\n\t"
      "v_mov_b32 v150, 0\n\t"
      "v_mov_b32 v151, 0\n\t"
      "v_mov_b32 v153, 0\n\t"
      "v_mov_b32 v154, 0\n\t"
      "v_mov_b32 v155, 0\n\t"
      "v_mov_b32 v157, 0\n\t"
      "v_mov_b32 v158, 0\n\t"
      "v_mov_b32 v159, 0\n\t"
      "v_mov_b32 v28, 0x3f80\n\t"
      "v_cmp_eq_u32 vcc, 0, v3\n\t"
      "v_mov_b32 v161, 0\n\t"
      "v_mov_b32 v162, 0\n\t"
      "v_cndmask_b32 v160, 0, v28, vcc\n\t"
      "v_mov_b32 v163, 0\n\t"
      "v_mfma_f32_32x32x16_bf16 v[64:79], v[88:91], v[116:119], 0\n\t"
      "ds_read_b64_tr_b16 v[112:113], v16 offset:6272\n\t"
      "ds_read_b64_tr_b16 v[114:115], v16 offset:7040\n\t"
      "buffer_load_dwordx4 v5, s[24:27], s42 offen offset:1024 lds\n\t"
      "s_waitcnt lgkmcnt(6)\n\t"
      "v_mfma_f32_32x32x16_bf16 v[32:47], v[92:95], v[120:123], v[32:47]\n\t"
      "ds_read_b64_tr_b16 v[80:81], v16 offset:9216\n\t"
      "ds_read_b64_tr_b16 v[82:83], v16 offset:9984\n\t"
      "buffer_load_dwordx4 v6, s[24:27], s42 offen offset:2048 lds\n\t"
      "v_mfma_f32_32x32x16_bf16 v[48:63], v[96:99], v[120:123], v[48:63]\n\t"
      "ds_read_b64_tr_b16 v[84:85], v16 offset:9280\n\t"
      "ds_read_b64_tr_b16 v[86:87], v16 offset:10048\n\t"
      "s_add_u32 s57, s99, 3\n\t"
      "s_and_b32 s57, s57, 7\n\t"
      "s_lshl_b32 s43, s57, 7\n\t"
      "s_add_u32 s43, s43, s93\n\t"
      "s_mul_i32 s51, s57, 176128\n\t"
      "s_add_u32 s51, s51, s98\n\t"
      "buffer_load_dwordx4 v7, s[24:27], s42 offen offset:3072 lds\n\t"
      "v_mfma_f32_32x32x16_bf16 v[64:79], v[100:103], v[120:123], v[64:79]\n\t"
      "ds_read_b64_tr_b16 v[88:89], v16 offset:9344\n\t"
      "ds_read_b64_tr_b16 v[90:91], v16 offset:10112\n\t"
      "s_waitcnt lgkmcnt(6)\n\t"
      "s_waitcnt vmcnt(12)\n\t"
      "v_mov_b32 v182, 3\n\t"
      "ds_write_b32 v181, v182\n\t"
      "s_mov_b32 s88, 0x400000\n\t"
      "46:\n\t"
      "ds_read_b128 v[184:187], v180\n\t"
      "s_waitcnt lgkmcnt(0)\n\t"
      "v_min_u32 v184, v184, v185\n\t"
      "v_min3_u32 v184, v184, v186, v187\n\t"
      "v_cmp_gt_u32 vcc, 3, v184\n\t"
      "s_sub_u32 s88, s88, 1\n\t"
      "s_cbranch_scc1 47f\n\t"
      "s_nop 2\n\t"
      "s_cbranch_vccnz 46b\n\t"
      "47:\n\t"
      "v_mfma_f32_32x32x16_bf16 v[32:47], v[104:107], v[124:127], v[32:47]\n\t"
      "ds_read_b64_tr_b16 v[92:93], v16 offset:12288\n\t"
      "ds_read_b64_tr_b16 v[94:95], v16 offset:13056\n\t"
      "ds_read_b128 v[132:135], v12 offset:4096\n\t"
      "ds_read_b128 v[136:139], v13 offset:4096\n\t"
      "v_mfma_f32_32x32x16_bf16 v[48:63], v[108:111], v[124:127], v[48:63]\n\t"
      "ds_read_b128 v[140:143], v14 offset:4096\n\t"
      "ds_read_b128 v[144:147], v15 offset:4096\n\t"
      "ds_read_b64_tr_b16 v[96:97], v16 offset:12352\n\t"
      "s_add_u32 s57, s99, 4\n\t"
      "s_and_b32 s57, s57, 7\n\t"
      "s_lshl_b32 s44, s57, 7\n\t"
      "s_add_u32 s44, s44, s93\n\t"
      "s_mul_i32 s52, s57, 176128\n\t"
      "s_add_u32 s52, s52, s98\n\t"
      "v_mfma_f32_32x32x16_bf16 v[64:79], v[112:115], v[124:127], v[64:79]\n\t"
      "ds_read_b64_tr_b16 v[98:99], v16 offset:13120\n\t"
      "ds_read_b64_tr_b16 v[100:101], v16 offset:12416\n\t"
      "ds_read_b64_tr_b16 v[102:103], v16 offset:13184\n\t"
      "v_mfma_f32_32x32x16_bf16 v[32:47], v[80:83], v[128:131], v[32:47]\n\t"
      "ds_read_b64_tr_b16 v[104:105], v16 offset:15360\n\t"
      "ds_read_b64_tr_b16 v[106:107], v16 offset:16128\n\t"
      "s_add_u32 m0, s61, 36864\n\t"
      "s_nop 0\n\t"
      "buffer_load_dwordx4 v8, s[28:31], s51 offen lds\n\t"
      "v_mfma_f32_32x32x16_bf16 v[48:63], v[84:87], v[128:131], v[48:63]\n\t"
      "ds_read_b64_tr_b16 v[108:109], v16 offset:15424\n\t"
      "ds_read_b64_tr_b16 v[110:111], v16 offset:16192\n\t"
      "s_add_u32 s57, s99, 5\n\t"
      "s_and_b32 s57, s57, 7\n\t"
      "s_lshl_b32 s45, s57, 7\n\t"
      "s_add_u32 s45, s45, s93\n\t"
      "s_mul_i32 s53, s57, 176128\n\t"
      "s_add_u32 s53, s53, s98\n\t"
      "buffer_load_dwordx4 v9, s[28:31], s51 offen offset:1024 lds\n\t"
      "v_mfma_f32_32x32x16_bf16 v[64:79], v[88:91], v[128:131], v[64:79]\n\t"
      "ds_read_b64_tr_b16 v[112:113], v16 offset:15488\n\t"
      "ds_read_b64_tr_b16 v[114:115], v16 offset:16256\n\t"
      "buffer_load_dwordx4 v10, s[28:31], s51 offen offset:2048 lds\n\t"
      "s_waitcnt lgkmcnt(6)\n\t"
      "v_mfma_f32_32x32x16_bf16 v[32:47], v[92:95], v[132:135], v[32:47]\n\t"
      "ds_read_b64_tr_b16 v[80:81], v16 offset:18432\n\t"
      "ds_read_b64_tr_b16 v[82:83], v16 offset:19200\n\t"
      "s_add_u32 m0, s60, 4096\n\t"
      "s_nop 0\n\t"
      "buffer_load_dwordx4 v4, s[24:27], s43 offen lds\n\t"
      "v_mfma_f32_32x32x16_bf16 v[48:63], v[96:99], v[132:135], v[48:63]\n\t"
      "ds_read_b64_tr_b16 v[84:85], v16 offset:18496\n\t"
      "ds_read_b64_tr_b16 v[86:87], v16 offset:19264\n\t"
      "s_add_u32 s57, s99, 6\n\t"
      "s_and_b32 s57, s57, 7\n\t"
      "s_lshl_b32 s46, s57, 7\n\t"
      "s_add_u32 s46, s46, s93\n\t"
      "s_mul_i32 s54, s57, 176128\n\t"
      "s_add_u32 s54, s54, s98\n\t"
      "v_mfma_f32_32x32x16_bf16 v[64:79], v[100:103], v[132:135], v[64:79]\n\t"
      "ds_read_b64_tr_b16 v[88:89], v16 offset:18560\n\t"
      "ds_read_b64_tr_b16 v[90:91], v16 offset:19328\n\t"
      "buffer_load_dwordx4 v5, s[24:27], s43 offen offset:1024 lds\n\t"
      "s_waitcnt lgkmcnt(6)\n\t"
      "v_mfma_f32_32x32x16_bf16 v[32:47], v[104:107], v[136:139], v[32:47]\n\t"
      "ds_read_b64_tr_b16 v[92:93], v16 offset:21504\n\t"
      "ds_read_b64_tr_b16 v[94:95], v16 offset:22272\n\t"
      "buffer_load_dwordx4 v6, s[24:27], s43 offen offset:2048 lds\n\t"
      "v_mfma_f32_32x32x16_bf16 v[48:63], v[108:111], v[136:139], v[48:63]\n\t"
      "ds_read_b64_tr_b16 v[96:97], v16 offset:21568\n\t"
      "ds_read_b64_tr_b16 v[98:99], v16 offset:22336\n\t"
      "s_add_u32 s57, s99, 7\n\t"
      "s_and_b32 s57, s57, 7\n\t"
      "s_lshl_b32 s47, s57, 7\n\t"
      "s_add_u32 s47, s47, s93\n\t"
      "s_mul_i32 s55, s57, 176128\n\t"
      "s_add_u32 s55, s55, s98\n\t"
      "buffer_load_dwordx4 v7, s[24:27], s43 offen offset:3072 lds\n\t"
      "v_mfma_f32_32x32x16_bf16 v[64:79], v[112:115], v[136:139], v[64:79]\n\t"
      "ds_read_b64_tr_b16 v[100:101], v16 offset:21632\n\t"
      "ds_read_b64_tr_b16 v[102:103], v16 offset:22400\n\t"
      "s_waitcnt lgkmcnt(6)\n\t"
      "s_waitcnt vmcnt(7)\n\t"
      "s_barrier\n\t"
      "s_mov_b64 s[58:59], exec\n\t"
      "s_mov_b64 exec, 1\n\t"
      "s_nop 0\n\t"
      "global_store_dwordx2 v23, v[24:25], s[86:87] sc1\n\t"
      "s_mov_b64 exec, s[58:59]\n\t"
      "v_mfma_f32_32x32x16_bf16 v[32:47], v[80:83], v[140:143], v[32:47]\n\t"
      "ds_read_b64_tr_b16 v[104:105], v16 offset:24576\n\t"
      "ds_read_b64_tr_b16 v[106:107], v16 offset:25344\n\t"
      "ds_read_b128 v[116:119], v12 offset:0\n\t"
      "ds_read_b128 v[120:123], v13 offset:0\n\t"
      "v_mfma_f32_32x32x16_bf16 v[48:63], v[84:87], v[140:143], v[48:63]\n\t"
      "ds_read_b128 v[124:127], v14 offset:0\n\t"
      "ds_read_b128 v[128:131], v15 offset:0\n\t"
      "ds_read_b64_tr_b16 v[108:109], v16 offset:24640\n\t"
      "v_mfma_f32_32x32x16_bf16 v[64:79], v[88:91], v[140:143], v[64:79]\n\t"
      "ds_read_b64_tr_b16 v[110:111], v16 offset:25408\n\t"
      "ds_read_b64_tr_b16 v[112:113], v16 offset:24704\n\t"
      "ds_read_b64_tr_b16 v[114:115], v16 offset:25472\n\t"
      "s_waitcnt lgkmcnt(10)\n\t"
      "v_mfma_f32_32x32x16_bf16 v[32:47], v[92:95], v[144:147], v[32:47]\n\t"
      "ds_read_b64_tr_b16 v[80:81], v16 offset:27648\n\t"
      "ds_read_b64_tr_b16 v[82:83], v16 offset:28416\n\t"
      "s_add_u32 m0, s61, 0\n\t"
      "s_nop 0\n\t"
      "buffer_load_dwordx4 v8, s[28:31], s52 offen lds\n\t"
      "v_mfma_f32_32x32x16_bf16 v[48:63], v[96:99], v[144:147], v[48:63]\n\t"
      "ds_read_b64_tr_b16 v[84:85], v16 offset:27712\n\t"
      "ds_read_b64_tr_b16 v[86:87], v16 offset:28480\n\t"
      "buffer_load_dwordx4 v9, s[28:31], s52 offen offset:1024 lds\n\t"
      "v_mfma_f32_32x32x16_bf16 v[64:79], v[100:103], v[144:147], v[64:79]\n\t"
      "ds_read_b64_tr_b16 v[88:89], v16 offset:27776\n\t"
      "ds_read_b64_tr_b16 v[90:91], v16 offset:28544\n\t"
      "buffer_load_dwordx4 v10, s[28:31], s52 offen offset:2048 lds\n\t"
      "v_mfma_f32_32x32x16_bf16 v[32:47], v[148:151], v[160:163], v[32:47]\n\t"
      "v_mfma_f32_32x32x16_bf16 v[48:63], v[152:155], v[160:163], v[48:63]\n\t"
      "v_mfma_f32_32x32x16_bf16 v[64:79], v[156:159], v[160:163], v[64:79]\n\t"
      "s_waitcnt lgkmcnt(6)\n\t"
      "v_mfma_f32_32x32x16_bf16 v[32:47], v[104:107], v[116:119], v[32:47]\n\t"
      "ds_read_b64_tr_b16 v[92:93], v16 offset:30720\n\t"
      "ds_read_b64_tr_b16 v[94:95], v16 offset:31488\n\t"
      "s_add_u32 m0, s60, 0\n\t"
      "s_nop 0\n\t"
      "buffer_load_dwordx4 v4, s[24:27], s44 offen lds\n\t"
      "v_mfma_f32_32x32x16_bf16 v[48:63], v[108:111], v[116:119], v[48:63]\n\t"
      "ds_read_b64_tr_b16 v[96:97], v16 offset:30784\n\t"
      "ds_read_b64_tr_b16 v[98:99], v16 offset:31552\n\t"
      "v_mfma_f32_32x32x16_bf16 v[64:79], v[112:115], v[116:119], v[64:79]\n\t"
      "ds_read_b64_tr_b16 v[100:101], v16 offset:30848\n\t"
      "ds_read_b64_tr_b16 v[102:103], v16 offset:31616\n\t"
      "buffer_load_dwordx4 v5, s[24:27], s44 offen offset:1024 lds\n\t"
      "s_waitcnt lgkmcnt(6)\n\t"
      "v_mfma_f32_32x32x16_bf16 v[32:47], v[80:83], v[120:123], v[32:47]\n\t"
      "ds_read_b64_tr_b16 v[104:105], v16 offset:33792\n\t"
      "ds_read_b64_tr_b16 v[106:107], v16 offset:34560\n\t"
      "buffer_load_dwordx4 v6, s[24:27], s44 offen offset:2048 lds\n\t"
      "v_mfma_f32_32x32x16_bf16 v[48:63], v[84:87], v[120:123], v[48:63]\n\t"
      "ds_read_b64_tr_b16 v[108:109], v16 offset:33856\n\t"
      "ds_read_b64_tr_b16 v[110:111], v16 offset:34624\n\t"
      "buffer_load_dwordx4 v7, s[24:27], s44 offen offset:3072 lds\n\t"
      "v_mfma_f32_32x32x16_bf16 v[64:79], v[88:91], v[120:123], v[64:79]\n\t"
      "ds_read_b64_tr_b16 v[112:113], v16 offset:33920\n\t"
      "ds_read_b64_tr_b16 v[114:115], v16 offset:34688\n\t"
      "s_waitcnt lgkmcnt(6)\n\t"
      "s_waitcnt vmcnt(8)\n\t"
      "s_barrier\n\t"
      "v_mfma_f32_32x32x16_bf16 v[32:47], v[92:95], v[124:127], v[32:47]\n\t"
      "ds_read_b64_tr_b16 v[80:81], v16 offset:36864\n\t"
      "ds_read_b64_tr_b16 v[82:83], v16 offset:37632\n\t"
      "ds_read_b128 v[132:135], v12 offset:4096\n\t"
      "ds_read_b128 v[136:139], v13 offset:4096\n\t"
      "v_mfma_f32_32x32x16_bf16 v[48:63], v[96:99], v[124:127], v[48:63]\n\t"
      "ds_read_b128 v[140:143], v14 offset:4096\n\t"
      "ds_read_b128 v[144:147], v15 offset:4096\n\t"
      "ds_read_b64_tr_b16 v[84:85], v16 offset:36928\n\t"
      "s_add_u32 s64, s86, 5242880\n\t"
      "s_addc_u32 s65, s87, 0\n\t"
      "s_and_b32 s65, s65, 0xffff\n\t"
      "s_mov_b32 s66, 5636096\n\t"
      "s_mov_b32 s67, 0x20000\n\t"
      "s_lshl2_add_u32 s62, s80, s84\n\t"
      "s_mul_i32 s92, s62, 86\n\t"
      "s_mul_i32 s63, s81, 6\n\t"
      "s_add_u32 s92, s92, s63\n\t"
      "s_lshl_b32 s92, s92, 10\n\t"
      "s_lshl_b32 s62, s62, 4\n\t"
      "s_add_u32 s62, s62, s81\n\t"
      "s_lshl_b32 s62, s62, 3\n\t"
      "s_add_u32 s62, s62, 16384\n\t"
      "v_mov_b32 v23, s62\n\t"
      "v_lshlrev_b32 v17, 4, v1\n\t"
      "v_mfma_f32_32x32x16_bf16 v[64:79], v[100:103], v[124:127], v[64:79]\n\t"
      "ds_read_b64_tr_b16 v[86:87], v16 offset:37696\n\t"
      "ds_read_b64_tr_b16 v[88:89], v16 offset:36992\n\t"
      "ds_read_b64_tr_b16 v[90:91], v16 offset:37760\n\t"
      "s_waitcnt lgkmcnt(10)\n\t"
      "v_mfma_f32_32x32x16_bf16 v[32:47], v[104:107], v[128:131], v[32:47]\n\t"
      "ds_read_b64_tr_b16 v[92:93], v16 offset:39936\n\t"
      "ds_read_b64_tr_b16 v[94:95], v16 offset:40704\n\t"
      "s_add_u32 m0, s61, 12288\n\t"
      "s_nop 0\n\t"
      "buffer_load_dwordx4 v8, s[28:31], s53 offen lds\n\t"
      "v_bfe_u32 v28, v1, 2, 2\n\t"
      "v_lshl_add_u32 v29, v3, 2, v28\n\t"
      "v_lshlrev_b32 v20, 7, v29\n\t"
      "v_mfma_f32_32x32x16_bf16 v[48:63], v[108:111], v[128:131], v[48:63]\n\t"
      "ds_read_b64_tr_b16 v[96:97], v16 offset:40000\n\t"
      "ds_read_b64_tr_b16 v[98:99], v16 offset:40768\n\t"
      "buffer_load_dwordx4 v9, s[28:31], s53 offen offset:1024 lds\n\t"
      "v_bfe_u32 v27, v1, 4, 1\n\t"
      "v_bfe_u32 v21, v1, 1, 1\n\t"
      "v_lshl_add_u32 v21, v27, 1, v21\n\t"
      "v_mfma_f32_32x32x16_bf16 v[64:79], v[112:115], v[128:131], v[64:79]\n\t"
      "ds_read_b64_tr_b16 v[100:101], v16 offset:40064\n\t"
      "ds_read_b64_tr_b16 v[102:103], v16 offset:40832\n\t"
      "buffer_load_dwordx4 v10, s[28:31], s53 offen offset:2048 lds\n\t"
      "v_lshrrev_b32 v28, 1, v28\n\t"
      "v_lshlrev_b32 v28, 2, v28\n\t"
      "v_xor_b32 v21, v21, v28\n\t"
      "s_waitcnt lgkmcnt(6)\n\t"
      "v_mfma_f32_32x32x16_bf16 v[32:47], v[80:83], v[132:135], v[32:47]\n\t"
      "ds_read_b64_tr_b16 v[104:105], v16 offset:43008\n\t"
      "ds_read_b64_tr_b16 v[106:107], v16 offset:43776\n\t"
      "s_add_u32 m0, s60, 4096\n\t"
      "s_nop 0\n\t"
      "buffer_load_dwordx4 v4, s[24:27], s45 offen lds\n\t"
      "v_lshl_add_u32 v20, v21, 4, v20\n\t"
      "v_and_b32 v28, 1, v1\n\t"
      "v_lshl_add_u32 v20, v28, 3, v20\n\t"
      "v_mfma_f32_32x32x16_bf16 v[48:63], v[84:87], v[132:135], v[48:63]\n\t"
      "ds_read_b64_tr_b16 v[108:109], v16 offset:43072\n\t"
      "ds_read_b64_tr_b16 v[110:111], v16 offset:43840\n\t"
      "s_and_b32 s62, s84, 1\n\t"
      "s_lshl_b32 s62, s62, 6\n\t"
      "v_xor_b32 v20, s62, v20\n\t"
      "v_add_u32 v18, s91, v20\n\t"
      "v_xor_b32 v19, 64, v18\n\t"
      "v_mfma_f32_32x32x16_bf16 v[64:79], v[88:91], v[132:135], v[64:79]\n\t"
      "ds_read_b64_tr_b16 v[112:113], v16 offset:43136\n\t"
      "ds_read_b64_tr_b16 v[114:115], v16 offset:43904\n\t"
      "buffer_load_dwordx4 v5, s[24:27], s45 offen offset:1024 lds\n\t"
      "s_waitcnt lgkmcnt(6)\n\t"
      "v_mfma_f32_32x32x16_bf16 v[32:47], v[92:95], v[136:139], v[32:47]\n\t"
      "ds_read_b64_tr_b16 v[80:81], v16 offset:46080\n\t"
      "ds_read_b64_tr_b16 v[82:83], v16 offset:46848\n\t"
      "buffer_load_dwordx4 v6, s[24:27], s45 offen offset:2048 lds\n\t"
      "v_mfma_f32_32x32x16_bf16 v[48:63], v[96:99], v[136:139], v[48:63]\n\t"
      "ds_read_b64_tr_b16 v[84:85], v16 offset:46144\n\t"
      "ds_read_b64_tr_b16 v[86:87], v16 offset:46912\n\t"
      "buffer_load_dwordx4 v7, s[24:27], s45 offen offset:3072 lds\n\t"
      "v_mfma_f32_32x32x16_bf16 v[64:79], v[100:103], v[136:139], v[64:79]\n\t"
      "ds_read_b64_tr_b16 v[88:89], v16 offset:46208\n\t"
      "ds_read_b64_tr_b16 v[90:91], v16 offset:46976\n\t"
      "s_waitcnt lgkmcnt(6)\n\t"
      "s_waitcnt vmcnt(7)\n\t"
      "s_barrier\n\t"
      "v_mfma_f32_32x32x16_bf16 v[32:47], v[104:107], v[140:143], v[32:47]\n\t"
      "ds_read_b64_tr_b16 v[92:93], v16 offset:0\n\t"
      "ds_read_b64_tr_b16 v[94:95], v16 offset:768\n\t"
      "ds_read_b128 v[116:119], v12 offset:0\n\t"
      "ds_read_b128 v[120:123], v13 offset:0\n\t"
      "v_mfma_f32_32x32x16_bf16 v[48:63], v[108:111], v[140:143], v[48:63]\n\t"
      "ds_read_b128 v[124:127], v14 offset:0\n\t"
      "ds_read_b128 v[128:131], v15 offset:0\n\t"
      "ds_read_b64_tr_b16 v[96:97], v16 offset:64\n\t"
      "v_mfma_f32_32x32x16_bf16 v[64:79], v[112:115], v[140:143], v[64:79]\n\t"
      "ds_read_b64_tr_b16 v[98:99], v16 offset:832\n\t"
      "ds_read_b64_tr_b16 v[100:101], v16 offset:128\n\t"
      "ds_read_b64_tr_b16 v[102:103], v16 offset:896\n\t"
      "s_waitcnt lgkmcnt(10)\n\t"
      "v_mfma_f32_32x32x16_bf16 v[32:47], v[80:83], v[144:147], v[32:47]\n\t"
      "ds_read_b64_tr_b16 v[104:105], v16 offset:3072\n\t"
      "ds_read_b64_tr_b16 v[106:107], v16 offset:3840\n\t"
      "s_add_u32 m0, s61, 24576\n\t"
      "s_nop 0\n\t"
      "buffer_load_dwordx4 v8, s[28:31], s54 offen lds\n\t"
      "v_mfma_f32_32x32x16_bf16 v[48:63], v[84:87], v[144:147], v[48:63]\n\t"
      "ds_read_b64_tr_b16 v[108:109], v16 offset:3136\n\t"
      "ds_read_b64_tr_b16 v[110:111], v16 offset:3904\n\t"
      "buffer_load_dwordx4 v9, s[28:31], s54 offen offset:1024 lds\n\t"
      "v_mfma_f32_32x32x16_bf16 v[64:79], v[88:91], v[144:147], v[64:79]\n\t"
      "ds_read_b64_tr_b16 v[112:113], v16 offset:3200\n\t"
      "ds_read_b64_tr_b16 v[114:115], v16 offset:3968\n\t"
      "buffer_load_dwordx4 v10, s[28:31], s54 offen offset:2048 lds\n\t"
      "s_waitcnt lgkmcnt(6)\n\t"
      "v_mfma_f32_32x32x16_bf16 v[32:47], v[92:95], v[116:119], v[32:47]\n\t"
      "ds_read_b64_tr_b16 v[80:81], v16 offset:6144\n\t"
      "ds_read_b64_tr_b16 v[82:83], v16 offset:6912\n\t"
      "s_add_u32 m0, s60, 0\n\t"
      "s_nop 0\n\t"
      "buffer_load_dwordx4 v4, s[24:27], s46 offen lds\n\t"
      "v_mfma_f32_32x32x16_bf16 v[48:63], v[96:99], v[116:119], v[48:63]\n\t"
      "ds_read_b64_tr_b16 v[84:85], v16 offset:6208\n\t"
      "ds_read_b64_tr_b16 v[86:87], v16 offset:6976\n\t"
      "v_mfma_f32_32x32x16_bf16 v[64:79], v[100:103], v[116:119], v[64:79]\n\t"
      "ds_read_b64_tr_b16 v[88:89], v16 offset:6272\n\t"
      "ds_read_b64_tr_b16 v[90:91], v16 offset:7040\n\t"
      "buffer_load_dwordx4 v5, s[24:27], s46 offen offset:1024 lds\n\t"
      "s_waitcnt lgkmcnt(6)\n\t"
      "v_mfma_f32_32x32x16_bf16 v[32:47], v[104:107], v[120:123], v[32:47]\n\t"
      "ds_read_b64_tr_b16 v[92:93], v16 offset:9216\n\t"
      "ds_read_b64_tr_b16 v[94:95], v16 offset:9984\n\t"
      "buffer_load_dwordx4 v6, s[24:27], s46 offen offset:2048 lds\n\t"
      "v_mfma_f32_32x32x16_bf16 v[48:63], v[108:111], v[120:123], v[48:63]\n\t"
      "ds_read_b64_tr_b16 v[96:97], v16 offset:9280\n\t"
      "ds_read_b64_tr_b16 v[98:99], v16 offset:10048\n\t"
      "buffer_load_dwordx4 v7, s[24:27], s46 offen offset:3072 lds\n\t"
      "v_mfma_f32_32x32x16_bf16 v[64:79], v[112:115], v[120:123], v[64:79]\n\t"
      "ds_read_b64_tr_b16 v[100:101], v16 offset:9344\n\t"
      "ds_read_b64_tr_b16 v[102:103], v16 offset:10112\n\t"
      "s_waitcnt lgkmcnt(6)\n\t"
      "s_waitcnt vmcnt(7)\n\t"
      "s_barrier\n\t"
      "v_mfma_f32_32x32x16_bf16 v[32:47], v[80:83], v[124:127], v[32:47]\n\t"
      "ds_read_b64_tr_b16 v[104:105], v16 offset:12288\n\t"
      "ds_read_b64_tr_b16 v[106:107], v16 offset:13056\n\t"
      "ds_read_b128 v[132:135], v12 offset:4096\n\t"
      "ds_read_b128 v[136:139], v13 offset:4096\n\t"
      "v_mfma_f32_32x32x16_bf16 v[48:63], v[84:87], v[124:127], v[48:63]\n\t"
      "ds_read_b128 v[140:143], v14 offset:4096\n\t"
      "ds_read_b128 v[144:147], v15 offset:4096\n\t"
      "ds_read_b64_tr_b16 v[108:109], v16 offset:12352\n\t"
      "v_mfma_f32_32x32x16_bf16 v[64:79], v[88:91], v[124:127], v[64:79]\n\t"
      "ds_read_b64_tr_b16 v[110:111], v16 offset:13120\n\t"
      "ds_read_b64_tr_b16 v[112:113], v16 offset:12416\n\t"
      "ds_read_b64_tr_b16 v[114:115], v16 offset:13184\n\t"
      "s_waitcnt lgkmcnt(10)\n\t"
      "v_mfma_f32_32x32x16_bf16 v[32:47], v[92:95], v[128:131], v[32:47]\n\t"
      "ds_read_b64_tr_b16 v[80:81], v16 offset:15360\n\t"
      "ds_read_b64_tr_b16 v[82:83], v16 offset:16128\n\t"
      "s_add_u32 m0, s61, 36864\n\t"
      "s_nop 0\n\t"
      "buffer_load_dwordx4 v8, s[28:31], s55 offen lds\n\t"
      "v_mfma_f32_32x32x16_bf16 v[48:63], v[96:99], v[128:131], v[48:63]\n\t"
      "ds_read_b64_tr_b16 v[84:85], v16 offset:15424\n\t"
      "ds_read_b64_tr_b16 v[86:87], v16 offset:16192\n\t"
      "buffer_load_dwordx4 v9, s[28:31], s55 offen offset:1024 lds\n\t"
      "v_mfma_f32_32x32x16_bf16 v[64:79], v[100:103], v[128:131], v[64:79]\n\t"
      "ds_read_b64_tr_b16 v[88:89], v16 offset:15488\n\t"
      "ds_read_b64_tr_b16 v[90:91], v16 offset:16256\n\t"
      "buffer_load_dwordx4 v10, s[28:31], s55 offen offset:2048 lds\n\t"
      "s_waitcnt lgkmcnt(6)\n\t"
      "v_mfma_f32_32x32x16_bf16 v[32:47], v[104:107], v[132:135], v[32:47]\n\t"
      "ds_read_b64_tr_b16 v[92:93], v16 offset:18432\n\t"
      "ds_read_b64_tr_b16 v[94:95], v16 offset:19200\n\t"
      "s_add_u32 m0, s60, 4096\n\t"
      "s_nop 0\n\t"
      "buffer_load_dwordx4 v4, s[24:27], s47 offen lds\n\t"
      "v_mfma_f32_32x32x16_bf16 v[48:63], v[108:111], v[132:135], v[48:63]\n\t"
      "ds_read_b64_tr_b16 v[96:97], v16 offset:18496\n\t"
      "ds_read_b64_tr_b16 v[98:99], v16 offset:19264\n\t"
      "v_mfma_f32_32x32x16_bf16 v[64:79], v[112:115], v[132:135], v[64:79]\n\t"
      "ds_read_b64_tr_b16 v[100:101], v16 offset:18560\n\t"
      "ds_read_b64_tr_b16 v[102:103], v16 offset:19328\n\t"
      "buffer_load_dwordx4 v5, s[24:27], s47 offen offset:1024 lds\n\t"
      "s_waitcnt lgkmcnt(6)\n\t"
      "v_mfma_f32_32x32x16_bf16 v[32:47], v[80:83], v[136:139], v[32:47]\n\t"
      "ds_read_b64_tr_b16 v[104:105], v16 offset:21504\n\t"
      "ds_read_b64_tr_b16 v[106:107], v16 offset:22272\n\t"
      "buffer_load_dwordx4 v6, s[24:27], s47 offen offset:2048 lds\n\t"
      "v_mfma_f32_32x32x16_bf16 v[48:63], v[84:87], v[136:139], v[48:63]\n\t"
      "ds_read_b64_tr_b16 v[108:109], v16 offset:21568\n\t"
      "ds_read_b64_tr_b16 v[110:111], v16 offset:22336\n\t"
      "buffer_load_dwordx4 v7, s[24:27], s47 offen offset:3072 lds\n\t"
      "v_mfma_f32_32x32x16_bf16 v[64:79], v[88:91], v[136:139], v[64:79]\n\t"
      "ds_read_b64_tr_b16 v[112:113], v16 offset:21632\n\t"
      "ds_read_b64_tr_b16 v[114:115], v16 offset:22400\n\t"
      "s_waitcnt lgkmcnt(6)\n\t"
      "s_waitcnt vmcnt(7)\n\t"
      "s_barrier\n\t"
      "v_mfma_f32_32x32x16_bf16 v[32:47], v[92:95], v[140:143], v[32:47]\n\t"
      "ds_read_b64_tr_b16 v[80:81], v16 offset:24576\n\t"
      "ds_read_b64_tr_b16 v[82:83], v16 offset:25344\n\t"
      "ds_read_b128 v[116:119], v12 offset:0\n\t"
      "ds_read_b128 v[120:123], v13 offset:0\n\t"
      "v_xor_b32 v5, 64, v20\n\t"
      "v_add_u32 v6, s61, v20\n\t"
      "v_add_u32 v7, s61, v5\n\t"
      "v_add_u32 v4, s60, v20\n\t"
      "v_add_u32 v5, s60, v5\n\t"
      "s_and_b32 s48, s84, 1\n\t"
      "v_lshlrev_b32 v21, 6, v29\n\t"
      "v_lshl_add_u32 v21, v27, 5, v21\n\t"
      "v_and_b32 v28, 3, v1\n\t"
      "v_mfma_f32_32x32x16_bf16 v[48:63], v[96:99], v[140:143], v[48:63]\n\t"
      "ds_read_b128 v[124:127], v14 offset:0\n\t"
      "ds_read_b128 v[128:131], v15 offset:0\n\t"
      "ds_read_b64_tr_b16 v[84:85], v16 offset:24640\n\t"
      "v_lshl_add_u32 v21, v28, 3, v21\n\t"
      "s_lshr_b32 s49, s84, 1\n\t"
      "s_mul_i32 s50, s49, 88064\n\t"
      "v_add_u32 v8, s50, v17\n\t"
      "s_sub_u32 s50, 88064, s50\n\t"
      "v_add_u32 v9, s50, v17\n\t"
      "s_lshl1_add_u32 s40, s83, s48\n\t"
      "s_lshl_b32 s40, s40, 5\n\t"
      "s_lshl1_add_u32 s41, s82, s49\n\t"
      "v_mfma_f32_32x32x16_bf16 v[64:79], v[100:103], v[140:143], v[64:79]\n\t"
      "ds_read_b64_tr_b16 v[86:87], v16 offset:25408\n\t"
      "ds_read_b64_tr_b16 v[88:89], v16 offset:24704\n\t"
      "ds_read_b64_tr_b16 v[90:91], v16 offset:25472\n\t"
      "s_lshl_b32 s41, s41, 5\n\t"
      "v_lshrrev_b32 v19, 2, v1\n\t"
      "v_add_u32 v19, s40, v19\n\t"
      "v_lshlrev_b32 v19, 12, v19\n\t"
      "v_lshl_add_u32 v19, v28, 4, v19\n\t"
      "s_lshl_b32 s50, s41, 1\n\t"
      "v_add_u32 v19, s50, v19\n\t"
      "v_add_u32 v29, s40, v2\n\t"
      "s_lshr_b32 s50, s82, 4\n\t"
      "s_lshl_b32 s50, s50, 12\n\t"
      "s_waitcnt lgkmcnt(10)\n\t"
      "v_mfma_f32_32x32x16_bf16 v[32:47], v[104:107], v[144:147], v[32:47]\n\t"
      "ds_read_b64_tr_b16 v[92:93], v16 offset:27648\n\t"
      "ds_read_b64_tr_b16 v[94:95], v16 offset:28416\n\t"
      "s_add_u32 m0, s61, 0\n\t"
      "s_add_u32 s57, s56, 8192\n\t"
      "buffer_load_dwordx4 v11, s[32:35], s56 offen lds\n\t"
      "s_cmp_eq_u32 s57, s90\n\t"
      "s_cselect_b32 s56, s89, s57\n\t"
      "v_mfma_f32_32x32x16_bf16 v[48:63], v[108:111], v[144:147], v[48:63]\n\t"
      "ds_read_b64_tr_b16 v[96:97], v16 offset:27712\n\t"
      "ds_read_b64_tr_b16 v[98:99], v16 offset:28480\n\t"
      "s_add_u32 m0, s61, 1024\n\t"
      "s_add_u32 s57, s56, 8192\n\t"
      "buffer_load_dwordx4 v11, s[32:35], s56 offen lds\n\t"
      "s_cmp_eq_u32 s57, s90\n\t"
      "s_cselect_b32 s56, s89, s57\n\t"
      "v_mfma_f32_32x32x16_bf16 v[64:79], v[112:115], v[144:147], v[64:79]\n\t"
      "ds_read_b64_tr_b16 v[100:101], v16 offset:27776\n\t"
      "ds_read_b64_tr_b16 v[102:103], v16 offset:28544\n\t"
      "s_add_u32 m0, s61, 2048\n\t"
      "s_add_u32 s57, s56, 8192\n\t"
      "buffer_load_dwordx4 v11, s[32:35], s56 offen lds\n\t"
      "s_cmp_eq_u32 s57, s90\n\t"
      "s_cselect_b32 s56, s89, s57\n\t"
      "s_waitcnt lgkmcnt(6)\n\t"
      "v_mfma_f32_32x32x16_bf16 v[32:47], v[80:83], v[116:119], v[32:47]\n\t"
      "ds_read_b64_tr_b16 v[104:105], v16 offset:30720\n\t"
      "ds_read_b64_tr_b16 v[106:107], v16 offset:31488\n\t"
      "s_add_u32 m0, s60, 0\n\t"
      "s_add_u32 s57, s56, 8192\n\t"
      "buffer_load_dwordx4 v11, s[32:35], s56 offen lds\n\t"
      "s_cmp_eq_u32 s57, s90\n\t"
      "s_cselect_b32 s56, s89, s57\n\t"
      "v_mfma_f32_32x32x16_bf16 v[48:63], v[84:87], v[116:119], v[48:63]\n\t"
      "ds_read_b64_tr_b16 v[108:109], v16 offset:30784\n\t"
      "ds_read_b64_tr_b16 v[110:111], v16 offset:31552\n\t"
      "s_add_u32 s50, s50, 24576\n\t"
      "v_lshlrev_b32 v18, 3, v29\n\t"
      "v_add_u32 v18, s50, v18\n\t"
      "v_lshlrev_b32 v26, 1, v29\n\t"
      "v_xor_b32 v20, 64, v26\n\t"
      "v_add_u32 v29, s41, v2\n\t"
      "v_lshlrev_b32 v31, 2, v29\n\t"
      "v_lshlrev_b32 v22, 10, v29\n\t"
      "v_lshl_add_u32 v22, v3, 4, v22\n\t"
      "v_mfma_f32_32x32x16_bf16 v[64:79], v[88:91], v[116:119], v[64:79]\n\t"
      "ds_read_b64_tr_b16 v[112:113], v16 offset:30848\n\t"
      "ds_read_b64_tr_b16 v[114:115], v16 offset:31616\n\t"
      "s_add_u32 m0, s60, 1024\n\t"
      "s_add_u32 s57, s56, 8192\n\t"
      "buffer_load_dwordx4 v11, s[32:35], s56 offen lds\n\t"
      "s_cmp_eq_u32 s57, s90\n\t"
      "s_cselect_b32 s56, s89, s57\n\t"
      "s_waitcnt lgkmcnt(6)\n\t"
      "v_mfma_f32_32x32x16_bf16 v[32:47], v[92:95], v[120:123], v[32:47]\n\t"
      "ds_read_b64_tr_b16 v[80:81], v16 offset:33792\n\t"
      "ds_read_b64_tr_b16 v[82:83], v16 offset:34560\n\t"
      "s_add_u32 m0, s60, 2048\n\t"
      "s_add_u32 s57, s56, 8192\n\t"
      "buffer_load_dwordx4 v11, s[32:35], s56 offen lds\n\t"
      "s_cmp_eq_u32 s57, s90\n\t"
      "s_cselect_b32 s56, s89, s57\n\t"
      "v_mfma_f32_32x32x16_bf16 v[48:63], v[96:99], v[120:123], v[48:63]\n\t"
      "ds_read_b64_tr_b16 v[84:85], v16 offset:33856\n\t"
      "ds_read_b64_tr_b16 v[86:87], v16 offset:34624\n\t"
      "s_add_u32 m0, s60, 3072\n\t"
      "s_add_u32 s57, s56, 8192\n\t"
      "buffer_load_dwordx4 v11, s[32:35], s56 offen lds\n\t"
      "s_cmp_eq_u32 s57, s90\n\t"
      "s_cselect_b32 s56, s89, s57\n\t"
      "v_mfma_f32_32x32x16_bf16 v[64:79], v[100:103], v[120:123], v[64:79]\n\t"
      "ds_read_b64_tr_b16 v[88:89], v16 offset:33920\n\t"
      "ds_read_b64_tr_b16 v[90:91], v16 offset:34688\n\t"
      "s_lshl_b32 s50, s40, 1\n\t"
      "v_add_u32 v22, s50, v22\n\t"
      "s_mul_i32 s48, s84, 11\n\t"
      "s_mul_i32 s49, s48, 43\n\t"
      "s_lshr_b32 s49, s49, 7\n\t"
      "s_add_u32 s48, s48, 10\n\t"
      "s_mul_i32 s48, s48, 43\n\t"
      "s_lshr_b32 s48, s48, 7\n\t"
      "s_min_u32 s48, s48, 14\n\t"
      "v_and_b32 v28, 15, v1\n\t"
      "s_waitcnt lgkmcnt(6)\n\t"
      "s_waitcnt vmcnt(7)\n\t"
      "s_barrier\n\t"
      "v_mfma_f32_32x32x16_bf16 v[32:47], v[104:107], v[124:127], v[32:47]\n\t"
      "ds_read_b64_tr_b16 v[92:93], v16 offset:36864\n\t"
      "ds_read_b64_tr_b16 v[94:95], v16 offset:37632\n\t"
      "ds_read_b128 v[132:135], v12 offset:4096\n\t"
      "ds_read_b128 v[136:139], v13 offset:4096\n\t"
      "v_max_u32 v29, s49, v28\n\t"
      "v_min_u32 v29, s48, v29\n\t"
      "v_cmp_eq_u32 vcc, 15, v28\n\t"
      "s_lshl_b32 s48, s82, 8\n\t"
      "s_add_u32 s48, s48, 16384\n\t"
      "v_cndmask_b32 v29, v29, v28, vcc\n\t"
      "v_bfe_u32 v28, v1, 4, 1\n\t"
      "v_lshl_add_u32 v29, v28, 4, v29\n\t"
      "v_lshlrev_b32 v10, 3, v29\n\t"
      "v_mfma_f32_32x32x16_bf16 v[48:63], v[108:111], v[124:127], v[48:63]\n\t"
      "ds_read_b128 v[140:143], v14 offset:4096\n\t"
      "ds_read_b128 v[144:147], v15 offset:4096\n\t"
      "ds_read_b64_tr_b16 v[96:97], v16 offset:36928\n\t"
      "v_add_u32 v10, s48, v10\n\t"
      "s_mul_i32 s43, s82, 172\n\t"
      "s_mul_i32 s48, s84, 22\n\t"
      "s_add_u32 s43, s43, s48\n\t"
      "s_lshl_b32 s43, s43, 10\n\t"
      "s_add_u32 s44, s43, 22528\n\t"
      "s_cmp_eq_u32 s84, 3\n\t"
      "s_cselect_b32 s48, 2048, 0\n\t"
      "s_sub_u32 s45, s44, s48\n\t"
      "s_lshl_b32 s48, s95, 10\n\t"
      "v_mfma_f32_32x32x16_bf16 v[64:79], v[112:115], v[124:127], v[64:79]\n\t"
      "ds_read_b64_tr_b16 v[98:99], v16 offset:37696\n\t"
      "ds_read_b64_tr_b16 v[100:101], v16 offset:36992\n\t"
      "ds_read_b64_tr_b16 v[102:103], v16 offset:37760\n\t"
      "s_add_u32 s42, s43, s48\n\t"
      "s_mov_b32 s36, s74\n\t"
      "s_and_b32 s37, s75, 0xffff\n\t"
      "s_mov_b32 s38, 1024\n\t"
      "s_mov_b32 s39, 0x20000\n\t"
      "s_add_u32 s68, s86, 3145728\n\t"
      "s_addc_u32 s69, s87, 0\n\t"
      "s_and_b32 s69, s69, 0xffff\n\t"
      "s_mov_b32 s70, 2097152\n\t"
      "s_mov_b32 s71, 0x20000\n\t"
      "s_add_u32 s28, s86, 32768\n\t"
      "s_addc_u32 s29, s87, 0\n\t"
      "s_waitcnt lgkmcnt(10)\n\t"
      "v_mfma_f32_32x32x16_bf16 v[32:47], v[80:83], v[128:131], v[32:47]\n\t"
      "ds_read_b64_tr_b16 v[104:105], v16 offset:39936\n\t"
      "ds_read_b64_tr_b16 v[106:107], v16 offset:40704\n\t"
      "s_add_u32 m0, s61, 12288\n\t"
      "s_add_u32 s57, s56, 8192\n\t"
      "buffer_load_dwordx4 v11, s[32:35], s56 offen lds\n\t"
      "s_cmp_eq_u32 s57, s90\n\t"
      "s_cselect_b32 s56, s89, s57\n\t"
      "v_mfma_f32_32x32x16_bf16 v[48:63], v[84:87], v[128:131], v[48:63]\n\t"
      "ds_read_b64_tr_b16 v[108:109], v16 offset:40000\n\t"
      "ds_read_b64_tr_b16 v[110:111], v16 offset:40768\n\t"
      "s_add_u32 m0, s61, 13312\n\t"
      "s_add_u32 s57, s56, 8192\n\t"
      "buffer_load_dwordx4 v11, s[32:35], s56 offen lds\n\t"
      "s_cmp_eq_u32 s57, s90\n\t"
      "s_cselect_b32 s56, s89, s57\n\t"
      "v_mfma_f32_32x32x16_bf16 v[64:79], v[88:91], v[128:131], v[64:79]\n\t"
      "ds_read_b64_tr_b16 v[112:113], v16 offset:40064\n\t"
      "ds_read_b64_tr_b16 v[114:115], v16 offset:40832\n\t"
      "s_add_u32 m0, s61, 14336\n\t"
      "s_add_u32 s57, s56, 8192\n\t"
      "buffer_load_dwordx4 v11, s[32:35], s56 offen lds\n\t"
      "s_cmp_eq_u32 s57, s90\n\t"
      "s_cselect_b32 s56, s89, s57\n\t"
      "s_waitcnt lgkmcnt(6)\n\t"
      "v_mfma_f32_32x32x16_bf16 v[32:47], v[92:95], v[132:135], v[32:47]\n\t"
      "ds_read_b64_tr_b16 v[80:81], v16 offset:43008\n\t"
      "ds_read_b64_tr_b16 v[82:83], v16 offset:43776\n\t"
      "s_add_u32 m0, s60, 4096\n\t"
      "s_add_u32 s57, s56, 8192\n\t"
      "buffer_load_dwordx4 v11, s[32:35], s56 offen lds\n\t"
      "s_cmp_eq_u32 s57, s90\n\t"
      "s_cselect_b32 s56, s89, s57\n\t"
      "v_mfma_f32_32x32x16_bf16 v[48:63], v[96:99], v[132:135], v[48:63]\n\t"
      "ds_read_b64_tr_b16 v[84:85], v16 offset:43072\n\t"
      "ds_read_b64_tr_b16 v[86:87], v16 offset:43840\n\t"
      "s_and_b32 s29, s29, 0xffff\n\t"
      "s_mov_b32 s30, 8192\n\t"
      "s_mov_b32 s31, 0x20000\n\t"
      "s_mov_b32 s24, s78\n\t"
      "s_and_b32 s25, s79, 0xffff\n\t"
      "s_mov_b32 s26, 2097152\n\t"
      "s_mov_b32 s27, 0x20000\n\t"
      "v_mov_b32 v28, 0x3e80\n\t"
      "v_cmp_eq_u32 vcc, 0, v3\n\t"
      "v_mfma_f32_32x32x16_bf16 v[64:79], v[100:103], v[132:135], v[64:79]\n\t"
      "ds_read_b64_tr_b16 v[88:89], v16 offset:43136\n\t"
      "ds_read_b64_tr_b16 v[90:91], v16 offset:43904\n\t"
      "s_add_u32 m0, s60, 5120\n\t"
      "s_add_u32 s57, s56, 8192\n\t"
      "buffer_load_dwordx4 v11, s[32:35], s56 offen lds\n\t"
      "s_cmp_eq_u32 s57, s90\n\t"
      "s_cselect_b32 s56, s89, s57\n\t"
      "s_waitcnt lgkmcnt(6)\n\t"
      "v_mfma_f32_32x32x16_bf16 v[32:47], v[104:107], v[136:139], v[32:47]\n\t"
      "ds_read_b64_tr_b16 v[92:93], v16 offset:46080\n\t"
      "ds_read_b64_tr_b16 v[94:95], v16 offset:46848\n\t"
      "s_add_u32 m0, s60, 6144\n\t"
      "s_add_u32 s57, s56, 8192\n\t"
      "buffer_load_dwordx4 v11, s[32:35], s56 offen lds\n\t"
      "s_cmp_eq_u32 s57, s90\n\t"
      "s_cselect_b32 s56, s89, s57\n\t"
      "v_mfma_f32_32x32x16_bf16 v[48:63], v[108:111], v[136:139], v[48:63]\n\t"
      "ds_read_b64_tr_b16 v[96:97], v16 offset:46144\n\t"
      "ds_read_b64_tr_b16 v[98:99], v16 offset:46912\n\t"
      "s_add_u32 m0, s60, 7168\n\t"
      "s_add_u32 s57, s56, 8192\n\t"
      "buffer_load_dwordx4 v11, s[32:35], s56 offen lds\n\t"
      "s_cmp_eq_u32 s57, s90\n\t"
      "s_cselect_b32 s56, s89, s57\n\t"
      "v_mfma_f32_32x32x16_bf16 v[64:79], v[112:115], v[136:139], v[64:79]\n\t"
      "ds_read_b64_tr_b16 v[100:101], v16 offset:46208\n\t"
      "ds_read_b64_tr_b16 v[102:103], v16 offset:46976\n\t"
      "v_mov_b32 v149, 0\n\t"
      "v_mov_b32 v150, 0\n\t"
      "v_mov_b32 v151, 0\n\t"
      "v_mov_b32 v153, 0\n\t"
      "v_mov_b32 v154, 0\n\t"
      "v_mov_b32 v155, 0\n\t"
      "v_mov_b32 v157, 0\n\t"
      "v_mov_b32 v158, 0\n\t"
      "v_mov_b32 v159, 0\n\t"
      "v_cndmask_b32 v156, 0, v28, vcc\n\t"
      "s_waitcnt lgkmcnt(6)\n\t"
      "v_mfma_f32_32x32x16_bf16 v[32:47], v[80:83], v[140:143], v[32:47]\n\t"
      "v_mfma_f32_32x32x16_bf16 v[48:63], v[84:87], v[140:143], v[48:63]\n\t"
      "v_mfma_f32_32x32x16_bf16 v[64:79], v[88:91], v[140:143], v[64:79]\n\t"
      "s_waitcnt lgkmcnt(0)\n\t"
      "v_mfma_f32_32x32x16_bf16 v[32:47], v[92:95], v[144:147], v[32:47]\n\t"
      "v_mfma_f32_32x32x16_bf16 v[48:63], v[96:99], v[144:147], v[48:63]\n\t"
      "v_mfma_f32_32x32x16_bf16 v[64:79], v[100:103], v[144:147], v[64:79]\n\t"
      "s_cmp_eq_u32 s81, 14\n\t"
      "s_cselect_b32 s63, 5636096, 0\n\t"
      "s_add_u32 s93, s92, 2048\n\t"
      "s_or_b32 s93, s93, s63\n\t"
      "s_add_u32 s98, s93, 2048\n\t"
      "s_nop 4\n\t"
      "s_setprio 3\n\t"
      "v_max_i32 v32, 0, v32\n\t"
      "v_max_i32 v33, 0, v33\n\t"
      "v_max_i32 v34, 0, v34\n\t"
      "v_max_i32 v35, 0, v35\n\t"
      "v_max_i32 v36, 0, v36\n\t"
      "v_max_i32 v37, 0, v37\n\t"
      "v_max_i32 v38, 0, v38\n\t"
      "v_max_i32 v39, 0, v39\n\t"
      "v_pk_mul_f32 v[32:33], v[32:33], v[32:33]\n\t"
      "v_pk_mul_f32 v[34:35], v[34:35], v[34:35]\n\t"
      "v_pk_mul_f32 v[36:37], v[36:37], v[36:37]\n\t"
      "v_pk_mul_f32 v[38:39], v[38:39], v[38:39]\n\t"
      "v_cvt_pk_bf16_f32 v116, v32, v33\n\t"
      "v_cvt_pk_bf16_f32 v117, v34, v35\n\t"
      "v_cvt_pk_bf16_f32 v118, v36, v37\n\t"
      "v_cvt_pk_bf16_f32 v119, v38, v39\n\t"
      "buffer_store_dwordx4 v[116:119], v17, s[64:67], s92 offen sc1\n\t"
      "v_max_i32 v40, 0, v40\n\t"
      "v_max_i32 v41, 0, v41\n\t"
      "v_max_i32 v42, 0, v42\n\t"
      "v_max_i32 v43, 0, v43\n\t"
      "v_max_i32 v44, 0, v44\n\t"
      "v_max_i32 v45, 0, v45\n\t"
      "v_max_i32 v46, 0, v46\n\t"
      "v_max_i32 v47, 0, v47\n\t"
      "v_pk_mul_f32 v[40:41], v[40:41], v[40:41]\n\t"
      "v_pk_mul_f32 v[42:43], v[42:43], v[42:43]\n\t"
      "v_pk_mul_f32 v[44:45], v[44:45], v[44:45]\n\t"
      "v_pk_mul_f32 v[46:47], v[46:47], v[46:47]\n\t"
      "v_cvt_pk_bf16_f32 v120, v40, v41\n\t"
      "v_cvt_pk_bf16_f32 v121, v42, v43\n\t"
      "v_cvt_pk_bf16_f32 v122, v44, v45\n\t"
      "v_cvt_pk_bf16_f32 v123, v46, v47\n\t"
      "buffer_store_dwordx4 v[120:123], v17, s[64:67], s92 offen offset:1024 sc1\n\t"
      "v_max_i32 v48, 0, v48\n\t"
      "v_max_i32 v49, 0, v49\n\t"
      "v_max_i32 v50, 0, v50\n\t"
      "v_max_i32 v51, 0, v51\n\t"
      "v_max_i32 v52, 0, v52\n\t"
      "v_max_i32 v53, 0, v53\n\t"
      "v_max_i32 v54, 0, v54\n\t"
      "v_max_i32 v55, 0, v55\n\t"
      "v_pk_mul_f32 v[48:49], v[48:49], v[48:49]\n\t"
      "v_pk_mul_f32 v[50:51], v[50:51], v[50:51]\n\t"
      "v_pk_mul_f32 v[52:53], v[52:53], v[52:53]\n\t"
      "v_pk_mul_f32 v[54:55], v[54:55], v[54:55]\n\t"
      "v_cvt_pk_bf16_f32 v116, v48, v49\n\t"
      "v_cvt_pk_bf16_f32 v117, v50, v51\n\t"
      "v_cvt_pk_bf16_f32 v118, v52, v53\n\t"
      "v_cvt_pk_bf16_f32 v119, v54, v55\n\t"
      "buffer_store_dwordx4 v[116:119], v17, s[64:67], s93 offen sc1\n\t"
      "v_max_i32 v56, 0, v56\n\t"
      "v_max_i32 v57, 0, v57\n\t"
      "v_max_i32 v58, 0, v58\n\t"
      "v_max_i32 v59, 0, v59\n\t"
      "v_max_i32 v60, 0, v60\n\t"
      "v_max_i32 v61, 0, v61\n\t"
      "v_max_i32 v62, 0, v62\n\t"
      "v_max_i32 v63, 0, v63\n\t"
      "v_pk_mul_f32 v[56:57], v[56:57], v[56:57]\n\t"
      "v_pk_mul_f32 v[58:59], v[58:59], v[58:59]\n\t"
      "v_pk_mul_f32 v[60:61], v[60:61], v[60:61]\n\t"
      "v_pk_mul_f32 v[62:63], v[62:63], v[62:63]\n\t"
      "v_cvt_pk_bf16_f32 v120, v56, v57\n\t"
      "v_cvt_pk_bf16_f32 v121, v58, v59\n\t"
      "v_cvt_pk_bf16_f32 v122, v60, v61\n\t"
      "v_cvt_pk_bf16_f32 v123, v62, v63\n\t"
      "buffer_store_dwordx4 v[120:123], v17, s[64:67], s93 offen offset:1024 sc1\n\t"
      "v_max_i32 v64, 0, v64\n\t"
      "v_max_i32 v65, 0, v65\n\t"
      "v_max_i32 v66, 0, v66\n\t"
      "v_max_i32 v67, 0, v67\n\t"
      "v_max_i32 v68, 0, v68\n\t"
      "v_max_i32 v69, 0, v69\n\t"
      "v_max_i32 v70, 0, v70\n\t"
      "v_max_i32 v71, 0, v71\n\t"
      "v_pk_mul_f32 v[64:65], v[64:65], v[64:65]\n\t"
      "v_pk_mul_f32 v[66:67], v[66:67], v[66:67]\n\t"
      "v_pk_mul_f32 v[68:69], v[68:69], v[68:69]\n\t"
      "v_pk_mul_f32 v[70:71], v[70:71], v[70:71]\n\t"
      "v_cvt_pk_bf16_f32 v116, v64, v65\n\t"
      "v_cvt_pk_bf16_f32 v117, v66, v67\n\t"
      "v_cvt_pk_bf16_f32 v118, v68, v69\n\t"
      "v_cvt_pk_bf16_f32 v119, v70, v71\n\t"
      "buffer_store_dwordx4 v[116:119], v17, s[64:67], s98 offen sc1\n\t"
      "v_max_i32 v72, 0, v72\n\t"
      "v_max_i32 v73, 0, v73\n\t"
      "v_max_i32 v74, 0, v74\n\t"
      "v_max_i32 v75, 0, v75\n\t"
      "v_max_i32 v76, 0, v76\n\t"
      "v_max_i32 v77, 0, v77\n\t"
      "v_max_i32 v78, 0, v78\n\t"
      "v_max_i32 v79, 0, v79\n\t"
      "v_pk_mul_f32 v[72:73], v[72:73], v[72:73]\n\t"
      "v_pk_mul_f32 v[74:75], v[74:75], v[74:75]\n\t"
      "v_pk_mul_f32 v[76:77], v[76:77], v[76:77]\n\t"
      "v_pk_mul_f32 v[78:79], v[78:79], v[78:79]\n\t"
      "v_cvt_pk_bf16_f32 v120, v72, v73\n\t"
      "v_cvt_pk_bf16_f32 v121, v74, v75\n\t"
      "v_cvt_pk_bf16_f32 v122, v76, v77\n\t"
      "v_cvt_pk_bf16_f32 v123, v78, v79\n\t"
      "buffer_store_dwordx4 v[120:123], v17, s[64:67], s98 offen offset:1024 sc1\n\t"
      "s_waitcnt lgkmcnt(0)\n\t"
      "s_barrier\n\t"
      "31:\n\t"
      "s_waitcnt vmcnt(6)\n\t"
      "s_waitcnt vmcnt(0)\n\t"
      "s_mov_b64 s[58:59], exec\n\t"
      "s_mov_b64 exec, 1\n\t"
      "s_nop 0\n\t"
      "global_store_dwordx2 v23, v[24:25], s[86:87] sc1\n\t"
      "s_mov_b64 exec, s[58:59]\n\t"
      "ds_read_b64_tr_b16 v[160:161], v6 offset:0\n\t"
      "ds_read_b64_tr_b16 v[162:163], v6 offset:1024\n\t"
      "ds_read_b64_tr_b16 v[164:165], v7 offset:0\n\t"
      "ds_read_b64_tr_b16 v[166:167], v7 offset:1024\n\t"
      "ds_read_b64_tr_b16 v[168:169], v6 offset:2048\n\t"
      "ds_read_b64_tr_b16 v[170:171], v4 offset:0\n\t"
      "ds_read_b64_tr_b16 v[172:173], v7 offset:2048\n\t"
      "ds_read_b64_tr_b16 v[174:175], v5 offset:0\n\t"
      "ds_read_b64_tr_b16 v[176:177], v4 offset:1024\n\t"
      "ds_read_b64_tr_b16 v[178:179], v4 offset:2048\n\t"
      "ds_read_b64_tr_b16 v[180:181], v5 offset:1024\n\t"
      "ds_read_b64_tr_b16 v[182:183], v5 offset:2048\n\t"
      "ds_read_b64_tr_b16 v[184:185], v4 offset:3072\n\t"
      "ds_read_b64_tr_b16 v[186:187], v6 offset:12288\n\t"
      "ds_read_b64_tr_b16 v[188:189], v5 offset:3072\n\t"
      "ds_read_b64_tr_b16 v[190:191], v7 offset:12288\n\t"
      "ds_read_b64_tr_b16 v[192:193], v6 offset:13312\n\t"
      "ds_read_b64_tr_b16 v[194:195], v6 offset:14336\n\t"
      "ds_read_b64_tr_b16 v[196:197], v7 offset:13312\n\t"
      "ds_read_b64_tr_b16 v[198:199], v7 offset:14336\n\t"
      "ds_read_b64_tr_b16 v[200:201], v4 offset:4096\n\t"
      "ds_read_b64_tr_b16 v[202:203], v4 offset:5120\n\t"
      "ds_read_b64_tr_b16 v[204:205], v5 offset:4096\n\t"
      "ds_read_b64_tr_b16 v[206:207], v5 offset:5120\n\t"
      "ds_read_b64_tr_b16 v[208:209], v4 offset:6144\n\t"
      "ds_read_b64_tr_b16 v[210:211], v4 offset:7168\n\t"
      "ds_read_b64_tr_b16 v[212:213], v5 offset:6144\n\t"
      "ds_read_b64_tr_b16 v[214:215], v5 offset:7168\n\t"
      "s_add_u32 m0, s61, 24576\n\t"
      "s_add_u32 s57, s56, 8192\n\t"
      "buffer_load_dwordx4 v11, s[32:35], s56 offen lds\n\t"
      "s_cmp_eq_u32 s57, s90\n\t"
      "s_cselect_b32 s56, s89, s57\n\t"
      "s_add_u32 m0, s61, 25600\n\t"
      "s_add_u32 s57, s56, 8192\n\t"
      "buffer_load_dwordx4 v11, s[32:35], s56 offen lds\n\t"
      "s_cmp_eq_u32 s57, s90\n\t"
      "s_cselect_b32 s56, s89, s57\n\t"
      "s_add_u32 m0, s61, 26624\n\t"
      "s_add_u32 s57, s56, 8192\n\t"
      "buffer_load_dwordx4 v11, s[32:35], s56 offen lds\n\t"
      "s_cmp_eq_u32 s57, s90\n\t"
      "s_cselect_b32 s56, s89, s57\n\t"
      "s_add_u32 m0, s61, 36864\n\t"
      "s_add_u32 s57, s56, 8192\n\t"
      "buffer_load_dwordx4 v11, s[32:35], s56 offen lds\n\t"
      "s_cmp_eq_u32 s57, s90\n\t"
      "s_cselect_b32 s56, s89, s57\n\t"
      "s_add_u32 m0, s61, 37888\n\t"
      "s_add_u32 s57, s56, 8192\n\t"
      "buffer_load_dwordx4 v11, s[32:35], s56 offen lds\n\t"
      "s_cmp_eq_u32 s57, s90\n\t"
      "s_cselect_b32 s56, s89, s57\n\t"
      "s_add_u32 m0, s61, 38912\n\t"
      "s_add_u32 s57, s56, 8192\n\t"
      "buffer_load_dwordx4 v11, s[32:35], s56 offen lds\n\t"
      "s_cmp_eq_u32 s57, s90\n\t"
      "s_cselect_b32 s56, s89, s57\n\t"
      "s_waitcnt lgkmcnt(0)\n\t"
      "buffer_load_ushort v148, v26, s[36:39], 0 offen\n\t"
      "buffer_load_ushort v152, v20, s[36:39], 0 offen\n\t"
      "s_add_u32 m0, s61, 0\n\t"
      "s_add_u32 s57, s56, 8192\n\t"
      "buffer_load_dwordx4 v11, s[32:35], s56 offen lds\n\t"
      "s_cmp_eq_u32 s57, s90\n\t"
      "s_cselect_b32 s56, s89, s57\n\t"
      "s_add_u32 m0, s61, 1024\n\t"
      "s_add_u32 s57, s56, 8192\n\t"
      "buffer_load_dwordx4 v11, s[32:35], s56 offen lds\n\t"
      "s_cmp_eq_u32 s57, s90\n\t"
      "s_cselect_b32 s56, s89, s57\n\t"
      "s_add_u32 m0, s61, 2048\n\t"
      "s_add_u32 s57, s56, 8192\n\t"
      "buffer_load_dwordx4 v11, s[32:35], s56 offen lds\n\t"
      "s_cmp_eq_u32 s57, s90\n\t"
      "s_cselect_b32 s56, s89, s57\n\t"
      "s_add_u32 m0, s60, 0\n\t"
      "s_add_u32 s57, s56, 8192\n\t"
      "buffer_load_dwordx4 v11, s[32:35], s56 offen lds\n\t"
      "s_cmp_eq_u32 s57, s90\n\t"
      "s_cselect_b32 s56, s89, s57\n\t"
      "s_add_u32 m0, s60, 1024\n\t"
      "s_add_u32 s57, s56, 8192\n\t"
      "buffer_load_dwordx4 v11, s[32:35], s56 offen lds\n\t"
      "s_cmp_eq_u32 s57, s90\n\t"
      "s_cselect_b32 s56, s89, s57\n\t"
      "s_add_u32 m0, s60, 2048\n\t"
      "s_add_u32 s57, s56, 8192\n\t"
      "buffer_load_dwordx4 v11, s[32:35], s56 offen lds\n\t"
      "s_cmp_eq_u32 s57, s90\n\t"
      "s_cselect_b32 s56, s89, s57\n\t"
      "s_add_u32 m0, s60, 3072\n\t"
      "s_add_u32 s57, s56, 8192\n\t"
      "buffer_load_dwordx4 v11, s[32:35], s56 offen lds\n\t"
      "s_cmp_eq_u32 s57, s90\n\t"
      "s_cselect_b32 s56, s89, s57\n\t"
      "s_add_u32 m0, s61, 12288\n\t"
      "s_add_u32 s57, s56, 8192\n\t"
      "buffer_load_dwordx4 v11, s[32:35], s56 offen lds\n\t"
      "s_cmp_eq_u32 s57, s90\n\t"
      "s_cselect_b32 s56, s89, s57\n\t"
      "s_add_u32 m0, s61, 13312\n\t"
      "s_add_u32 s57, s56, 8192\n\t"
      "buffer_load_dwordx4 v11, s[32:35], s56 offen lds\n\t"
      "s_cmp_eq_u32 s57, s90\n\t"
      "s_cselect_b32 s56, s89, s57\n\t"
      "s_add_u32 m0, s61, 14336\n\t"
      "s_add_u32 s57, s56, 8192\n\t"
      "buffer_load_dwordx4 v11, s[32:35], s56 offen lds\n\t"
      "s_cmp_eq_u32 s57, s90\n\t"
      "s_cselect_b32 s56, s89, s57\n\t"
      "s_add_u32 m0, s60, 4096\n\t"
      "s_add_u32 s57, s56, 8192\n\t"
      "buffer_load_dwordx4 v11, s[32:35], s56 offen lds\n\t"
      "s_cmp_eq_u32 s57, s90\n\t"
      "s_cselect_b32 s56, s89, s57\n\t"
      "s_add_u32 m0, s60, 5120\n\t"
      "s_add_u32 s57, s56, 8192\n\t"
      "buffer_load_dwordx4 v11, s[32:35], s56 offen lds\n\t"
      "s_cmp_eq_u32 s57, s90\n\t"
      "s_cselect_b32 s56, s89, s57\n\t"
      "s_add_u32 m0, s60, 6144\n\t"
      "s_add_u32 s57, s56, 8192\n\t"
      "buffer_load_dwordx4 v11, s[32:35], s56 offen lds\n\t"
      "s_cmp_eq_u32 s57, s90\n\t"
      "s_cselect_b32 s56, s89, s57\n\t"
      "s_add_u32 m0, s60, 7168\n\t"
      "s_add_u32 s57, s56, 8192\n\t"
      "buffer_load_dwordx4 v11, s[32:35], s56 offen lds\n\t"
      "s_cmp_eq_u32 s57, s90\n\t"
      "s_cselect_b32 s56, s89, s57\n\t"
      "global_load_dwordx2 v[28:29], v10, s[86:87] sc1\n\t"
      "s_waitcnt vmcnt(0)\n\t"
      "s_mov_b32 s88, 200000\n\t"
      "1:\n\t"
      "v_cmp_ne_u64 vcc, v[28:29], v[24:25]\n\t"
      "s_nop 4\n\t"
      "s_cbranch_vccz 2f\n\t"
      "s_sub_u32 s88, s88, 1\n\t"
      "s_cbranch_scc1 2f\n\t"
      "s_sleep 4\n\t"
      "global_load_dwordx2 v[28:29], v10, s[86:87] sc1\n\t"
      "s_waitcnt vmcnt(0)\n\t"
      "s_branch 1b\n\t"
      "2:\n\t"
      "s_setprio 1\n\t"
      "s_cmp_ge_u32 s42, s45\n\t"
      "s_cselect_b32 s62, 5636096, s42\n\t"
      "s_add_u32 s42, s42, 1024\n\t"
      "buffer_load_dwordx4 v[96:99], v8, s[64:67], s62 offen sc1\n\t"
      "s_cmp_eq_u32 s42, s44\n\t"
      "buffer_load_dwordx4 v[100:103], v9, s[64:67], s62 offen sc1\n\t"
      "s_cselect_b32 s42, s43, s42\n\t"
      "s_cmp_ge_u32 s42, s45\n\t"
      "s_cselect_b32 s62, 5636096, s42\n\t"
      "s_add_u32 s42, s42, 1024\n\t"
      "buffer_load_dwordx4 v[104:107], v8, s[64:67], s62 offen sc1\n\t"
      "s_cmp_eq_u32 s42, s44\n\t"
      "buffer_load_dwordx4 v[108:111], v9, s[64:67], s62 offen sc1\n\t"
      "s_cselect_b32 s42, s43, s42\n\t"
      "s_cmp_ge_u32 s42, s45\n\t"
      "s_cselect_b32 s62, 5636096, s42\n\t"
      "s_add_u32 s42, s42, 1024\n\t"
      "buffer_load_dwordx4 v[112:115], v8, s[64:67], s62 offen sc1\n\t"
      "s_cmp_eq_u32 s42, s44\n\t"
      "buffer_load_dwordx4 v[116:119], v9, s[64:67], s62 offen sc1\n\t"
      "s_cselect_b32 s42, s43, s42\n\t"
      "s_cmp_ge_u32 s42, s45\n\t"
      "s_cselect_b32 s62, 5636096, s42\n\t"
      "s_add_u32 s42, s42, 1024\n\t"
      "buffer_load_dwordx4 v[120:123], v8, s[64:67], s62 offen sc1\n\t"
      "s_cmp_eq_u32 s42, s44\n\t"
      "buffer_load_dwordx4 v[124:127], v9, s[64:67], s62 offen sc1\n\t"
      "s_cselect_b32 s42, s43, s42\n\t"
      "buffer_load_dword v31, v31, s[28:31], 0 offen sc1\n\t"
      "ds_read_b64_tr_b16 v[216:217], v6 offset:24576\n\t"
      "ds_read_b64_tr_b16 v[218:219], v6 offset:25600\n\t"
      "ds_read_b64_tr_b16 v[220:221], v7 offset:24576\n\t"
      "ds_read_b64_tr_b16 v[222:223], v7 offset:25600\n\t"
      "ds_read_b64_tr_b16 v[224:225], v6 offset:26624\n\t"
      "ds_read_b64_tr_b16 v[226:227], v6 offset:36864\n\t"
      "ds_read_b64_tr_b16 v[228:229], v7 offset:26624\n\t"
      "ds_read_b64_tr_b16 v[230:231], v7 offset:36864\n\t"
      "ds_read_b64_tr_b16 v[232:233], v6 offset:37888\n\t"
      "ds_read_b64_tr_b16 v[234:235], v6 offset:38912\n\t"
      "ds_read_b64_tr_b16 v[236:237], v7 offset:37888\n\t"
      "ds_read_b64_tr_b16 v[238:239], v7 offset:38912\n\t"
      "ds_read_b64_tr_b16 v[240:241], v6 offset:0\n\t"
      "ds_read_b64_tr_b16 v[242:243], v6 offset:1024\n\t"
      "ds_read_b64_tr_b16 v[244:245], v7 offset:0\n\t"
      "ds_read_b64_tr_b16 v[246:247], v7 offset:1024\n\t"
      "ds_read_b64_tr_b16 v[248:249], v6 offset:2048\n\t"
      "ds_read_b64_tr_b16 v[250:251], v4 offset:0\n\t"
      "ds_read_b64_tr_b16 v[252:253], v7 offset:2048\n\t"
      "ds_read_b64_tr_b16 v[254:255], v5 offset:0\n\t"
      "v_mfma_f32_32x32x16_bf16 v[32:47], v[148:151], v[156:159], 0\n\t"
      "v_mfma_f32_32x32x16_bf16 v[48:63], v[152:155], v[156:159], 0\n\t"
      "v_mfma_f32_32x32x16_bf16 v[64:79], v[148:151], v[156:159], 0\n\t"
      "v_mfma_f32_32x32x16_bf16 v[80:95], v[152:155], v[156:159], 0\n\t"
      "s_waitcnt lgkmcnt(0)\n\t"
      "s_waitcnt lgkmcnt(0)\n\t"
      "s_waitcnt vmcnt(8)\n\t"
      "v_mfma_f32_32x32x16_bf16 v[32:47], v[160:163], v[96:99], v[32:47]\n\t"
      "v_mfma_f32_32x32x16_bf16 v[48:63], v[164:167], v[96:99], v[48:63]\n\t"
      "s_waitcnt vmcnt(7)\n\t"
      "v_mfma_f32_32x32x16_bf16 v[64:79], v[160:163], v[100:103], v[64:79]\n\t"
      "s_cmp_ge_u32 s42, s45\n\t"
      "s_cselect_b32 s62, 5636096, s42\n\t"
      "s_add_u32 s42, s42, 1024\n\t"
      "buffer_load_dwordx4 v[96:99], v8, s[64:67], s62 offen sc1\n\t"
      "v_mfma_f32_32x32x16_bf16 v[80:95], v[164:167], v[100:103], v[80:95]\n\t"
      "s_cmp_eq_u32 s42, s44\n\t"
      "buffer_load_dwordx4 v[100:103], v9, s[64:67], s62 offen sc1\n\t"
      "s_cselect_b32 s42, s43, s42\n\t"
      "s_waitcnt vmcnt(8)\n\t"
      "v_mfma_f32_32x32x16_bf16 v[32:47], v[168:171], v[104:107], v[32:47]\n\t"
      "v_add_u32 v12, s60, v17\n\t"
      "v_add_u32 v13, s61, v17\n\t"
      "v_add_u32 v16, 4294885376, v12\n\t"
      "v_xor_b32 v14, 8192, v16\n\t"
      "v_mfma_f32_32x32x16_bf16 v[48:63], v[172:175], v[104:107], v[48:63]\n\t"
      "v_xor_b32 v15, 16384, v16\n\t"
      "v_add_u32 v14, 81920, v14\n\t"
      "v_add_u32 v15, 81920, v15\n\t"
      "s_xor_b32 s48, s84, 3\n\t"
      "s_waitcnt vmcnt(7)\n\t"
      "v_mfma_f32_32x32x16_bf16 v[64:79], v[168:171], v[108:111], v[64:79]\n\t"
      "s_cmp_ge_u32 s42, s45\n\t"
      "s_cselect_b32 s62, 5636096, s42\n\t"
      "s_add_u32 s42, s42, 1024\n\t"
      "buffer_load_dwordx4 v[104:107], v8, s[64:67], s62 offen sc1\n\t"
      "s_mul_i32 s48, s48, 3072\n\t"
      "s_add_u32 s48, s48, 114688\n\t"
      "v_add_u32 v16, s48, v17\n\t"
      "v_add_u32 v26, s61, v17\n\t"
      "v_add_u32 v21, s61, v21\n\t"
      "v_mfma_f32_32x32x16_bf16 v[80:95], v[172:175], v[108:111], v[80:95]\n\t"
      "s_cmp_eq_u32 s42, s44\n\t"
      "buffer_load_dwordx4 v[108:111], v9, s[64:67], s62 offen sc1\n\t"
      "s_cselect_b32 s42, s43, s42\n\t"
      "s_waitcnt vmcnt(8)\n\t"
      "v_mfma_f32_32x32x16_bf16 v[32:47], v[176:179], v[112:115], v[32:47]\n\t"
      "v_mfma_f32_32x32x16_bf16 v[48:63], v[180:183], v[112:115], v[48:63]\n\t"
      "s_waitcnt vmcnt(7)\n\t"
      "v_mfma_f32_32x32x16_bf16 v[64:79], v[176:179], v[116:119], v[64:79]\n\t"
      "s_cmp_ge_u32 s42, s45\n\t"
      "s_cselect_b32 s62, 5636096, s42\n\t"
      "s_add_u32 s42, s42, 1024\n\t"
      "buffer_load_dwordx4 v[112:115], v8, s[64:67], s62 offen sc1\n\t"
      "v_mfma_f32_32x32x16_bf16 v[80:95], v[180:183], v[116:119], v[80:95]\n\t"
      "s_cmp_eq_u32 s42, s44\n\t"
      "buffer_load_dwordx4 v[116:119], v9, s[64:67], s62 offen sc1\n\t"
      "s_cselect_b32 s42, s43, s42\n\t"
      "s_waitcnt vmcnt(8)\n\t"
      "v_mfma_f32_32x32x16_bf16 v[32:47], v[184:187], v[120:123], v[32:47]\n\t"
      "v_mfma_f32_32x32x16_bf16 v[48:63], v[188:191], v[120:123], v[48:63]\n\t"
      "s_waitcnt vmcnt(7)\n\t"
      "v_mfma_f32_32x32x16_bf16 v[64:79], v[184:187], v[124:127], v[64:79]\n\t"
      "s_cmp_ge_u32 s42, s45\n\t"
      "s_cselect_b32 s62, 5636096, s42\n\t"
      "s_add_u32 s42, s42, 1024\n\t"
      "buffer_load_dwordx4 v[120:123], v8, s[64:67], s62 offen sc1\n\t"
      "v_mfma_f32_32x32x16_bf16 v[80:95], v[188:191], v[124:127], v[80:95]\n\t"
      "s_cmp_eq_u32 s42, s44\n\t"
      "buffer_load_dwordx4 v[124:127], v9, s[64:67], s62 offen sc1\n\t"
      "s_cselect_b32 s42, s43, s42\n\t"
      "s_waitcnt vmcnt(7)\n\t"
      "v_mfma_f32_32x32x16_bf16 v[32:47], v[192:195], v[96:99], v[32:47]\n\t"
      "v_mfma_f32_32x32x16_bf16 v[48:63], v[196:199], v[96:99], v[48:63]\n\t"
      "s_waitcnt vmcnt(6)\n\t"
      "v_mfma_f32_32x32x16_bf16 v[64:79], v[192:195], v[100:103], v[64:79]\n\t"
      "s_cmp_ge_u32 s42, s45\n\t"
      "s_cselect_b32 s62, 5636096, s42\n\t"
      "s_add_u32 s42, s42, 1024\n\t"
      "buffer_load_dwordx4 v[96:99], v8, s[64:67], s62 offen sc1\n\t"
      "v_mfma_f32_32x32x16_bf16 v[80:95], v[196:199], v[100:103], v[80:95]\n\t"
      "s_cmp_eq_u32 s42, s44\n\t"
      "buffer_load_dwordx4 v[100:103], v9, s[64:67], s62 offen sc1\n\t"
      "s_cselect_b32 s42, s43, s42\n\t"
      "s_waitcnt vmcnt(7)\n\t"
      "v_mfma_f32_32x32x16_bf16 v[32:47], v[200:203], v[104:107], v[32:47]\n\t"
      "v_mfma_f32_32x32x16_bf16 v[48:63], v[204:207], v[104:107], v[48:63]\n\t"
      "s_waitcnt vmcnt(6)\n\t"
      "v_mfma_f32_32x32x16_bf16 v[64:79], v[200:203], v[108:111], v[64:79]\n\t"
      "s_cmp_ge_u32 s42, s45\n\t"
      "s_cselect_b32 s62, 5636096, s42\n\t"
      "s_add_u32 s42, s42, 1024\n\t"
      "buffer_load_dwordx4 v[104:107], v8, s[64:67], s62 offen sc1\n\t"
      "v_mfma_f32_32x32x16_bf16 v[80:95], v[204:207], v[108:111], v[80:95]\n\t"
      "s_cmp_eq_u32 s42, s44\n\t"
      "buffer_load_dwordx4 v[108:111], v9, s[64:67], s62 offen sc1\n\t"
      "s_cselect_b32 s42, s43, s42\n\t"
      "s_waitcnt vmcnt(7)\n\t"
      "v_mfma_f32_32x32x16_bf16 v[32:47], v[208:211], v[112:115], v[32:47]\n\t"
      "s_add_u32 m0, s61, 24576\n\t"
      "s_add_u32 s57, s56, 8192\n\t"
      "buffer_load_dwordx4 v11, s[32:35], s56 offen lds\n\t"
      "s_cmp_eq_u32 s57, s90\n\t"
      "s_cselect_b32 s56, s89, s57\n\t"
      "v_mfma_f32_32x32x16_bf16 v[48:63], v[212:215], v[112:115], v[48:63]\n\t"
      "s_add_u32 m0, s61, 25600\n\t"
      "s_add_u32 s57, s56, 8192\n\t"
      "buffer_load_dwordx4 v11, s[32:35], s56 offen lds\n\t"
      "s_cmp_eq_u32 s57, s90\n\t"
      "s_cselect_b32 s56, s89, s57\n\t"
      "s_waitcnt vmcnt(8)\n\t"
      "v_mfma_f32_32x32x16_bf16 v[64:79], v[208:211], v[116:119], v[64:79]\n\t"
      "s_cmp_ge_u32 s42, s45\n\t"
      "s_cselect_b32 s62, 5636096, s42\n\t"
      "s_add_u32 s42, s42, 1024\n\t"
      "buffer_load_dwordx4 v[112:115], v8, s[64:67], s62 offen sc1\n\t"
      "v_mfma_f32_32x32x16_bf16 v[80:95], v[212:215], v[116:119], v[80:95]\n\t"
      "s_cmp_eq_u32 s42, s44\n\t"
      "buffer_load_dwordx4 v[116:119], v9, s[64:67], s62 offen sc1\n\t"
      "s_cselect_b32 s42, s43, s42\n\t"
      "s_waitcnt vmcnt(9)\n\t"
      "v_mfma_f32_32x32x16_bf16 v[32:47], v[216:219], v[120:123], v[32:47]\n\t"
      "s_add_u32 m0, s61, 26624\n\t"
      "s_add_u32 s57, s56, 8192\n\t"
      "buffer_load_dwordx4 v11, s[32:35], s56 offen lds\n\t"
      "s_cmp_eq_u32 s57, s90\n\t"
      "s_cselect_b32 s56, s89, s57\n\t"
      "v_mfma_f32_32x32x16_bf16 v[48:63], v[220:223], v[120:123], v[48:63]\n\t"
      "s_add_u32 m0, s61, 36864\n\t"
      "s_add_u32 s57, s56, 8192\n\t"
      "buffer_load_dwordx4 v11, s[32:35], s56 offen lds\n\t"
      "s_cmp_eq_u32 s57, s90\n\t"
      "s_cselect_b32 s56, s89, s57\n\t"
      "s_waitcnt vmcnt(10)\n\t"
      "v_mfma_f32_32x32x16_bf16 v[64:79], v[216:219], v[124:127], v[64:79]\n\t"
      "s_cmp_ge_u32 s42, s45\n\t"
      "s_cselect_b32 s62, 5636096, s42\n\t"
      "s_add_u32 s42, s42, 1024\n\t"
      "buffer_load_dwordx4 v[120:123], v8, s[64:67], s62 offen sc1\n\t"
      "v_mfma_f32_32x32x16_bf16 v[80:95], v[220:223], v[124:127], v[80:95]\n\t"
      "s_cmp_eq_u32 s42, s44\n\t"
      "buffer_load_dwordx4 v[124:127], v9, s[64:67], s62 offen sc1\n\t"
      "s_cselect_b32 s42, s43, s42\n\t"
      "s_waitcnt vmcnt(11)\n\t"
      "v_mfma_f32_32x32x16_bf16 v[32:47], v[224:227], v[96:99], v[32:47]\n\t"
      "s_add_u32 m0, s61, 37888\n\t"
      "s_add_u32 s57, s56, 8192\n\t"
      "buffer_load_dwordx4 v11, s[32:35], s56 offen lds\n\t"
      "s_cmp_eq_u32 s57, s90\n\t"
      "s_cselect_b32 s56, s89, s57\n\t"
      "v_mfma_f32_32x32x16_bf16 v[48:63], v[228:231], v[96:99], v[48:63]\n\t"
      "s_add_u32 m0, s61, 38912\n\t"
      "s_add_u32 s57, s56, 8192\n\t"
      "buffer_load_dwordx4 v11, s[32:35], s56 offen lds\n\t"
      "s_cmp_eq_u32 s57, s90\n\t"
      "s_cselect_b32 s56, s89, s57\n\t"
      "s_waitcnt vmcnt(12)\n\t"
      "v_mfma_f32_32x32x16_bf16 v[64:79], v[224:227], v[100:103], v[64:79]\n\t"
      "s_cmp_ge_u32 s42, s45\n\t"
      "s_cselect_b32 s62, 5636096, s42\n\t"
      "s_add_u32 s42, s42, 1024\n\t"
      "buffer_load_dwordx4 v[96:99], v8, s[64:67], s62 offen sc1\n\t"
      "v_mfma_f32_32x32x16_bf16 v[80:95], v[228:231], v[100:103], v[80:95]\n\t"
      "s_cmp_eq_u32 s42, s44\n\t"
      "buffer_load_dwordx4 v[100:103], v9, s[64:67], s62 offen sc1\n\t"
      "s_cselect_b32 s42, s43, s42\n\t"
      "s_waitcnt vmcnt(13)\n\t"
      "v_mfma_f32_32x32x16_bf16 v[32:47], v[232:235], v[104:107], v[32:47]\n\t"
      "s_add_u32 m0, s61, 0\n\t"
      "s_add_u32 s57, s56, 8192\n\t"
      "buffer_load_dwordx4 v11, s[32:35], s56 offen lds\n\t"
      "s_cmp_eq_u32 s57, s90\n\t"
      "s_cselect_b32 s56, s89, s57\n\t"
      "v_mfma_f32_32x32x16_bf16 v[48:63], v[236:239], v[104:107], v[48:63]\n\t"
      "s_add_u32 m0, s61, 1024\n\t"
      "s_add_u32 s57, s56, 8192\n\t"
      "buffer_load_dwordx4 v11, s[32:35], s56 offen lds\n\t"
      "s_cmp_eq_u32 s57, s90\n\t"
      "s_cselect_b32 s56, s89, s57\n\t"
      "s_waitcnt vmcnt(14)\n\t"
      "v_mfma_f32_32x32x16_bf16 v[64:79], v[232:235], v[108:111], v[64:79]\n\t"
      "s_cmp_ge_u32 s42, s45\n\t"
      "s_cselect_b32 s62, 5636096, s42\n\t"
      "s_add_u32 s42, s42, 1024\n\t"
      "buffer_load_dwordx4 v[104:107], v8, s[64:67], s62 offen sc1\n\t"
      "v_mfma_f32_32x32x16_bf16 v[80:95], v[236:239], v[108:111], v[80:95]\n\t"
      "s_cmp_eq_u32 s42, s44\n\t"
      "buffer_load_dwordx4 v[108:111], v9, s[64:67], s62 offen sc1\n\t"
      "s_cselect_b32 s42, s43, s42\n\t"
      "s_waitcnt vmcnt(13)\n\t"
      "v_mfma_f32_32x32x16_bf16 v[32:47], v[240:243], v[112:115], v[32:47]\n\t"
      "s_add_u32 m0, s61, 2048\n\t"
      "s_add_u32 s57, s56, 8192\n\t"
      "buffer_load_dwordx4 v11, s[32:35], s56 offen lds\n\t"
      "s_cmp_eq_u32 s57, s90\n\t"
      "s_cselect_b32 s56, s89, s57\n\t"
      "v_mfma_f32_32x32x16_bf16 v[48:63], v[244:247], v[112:115], v[48:63]\n\t"
      "s_add_u32 m0, s60, 0\n\t"
      "s_add_u32 s57, s56, 8192\n\t"
      "buffer_load_dwordx4 v11, s[32:35], s56 offen lds\n\t"
      "s_cmp_eq_u32 s57, s90\n\t"
      "s_cselect_b32 s56, s89, s57\n\t"
      "s_waitcnt vmcnt(14)\n\t"
      "v_mfma_f32_32x32x16_bf16 v[64:79], v[240:243], v[116:119], v[64:79]\n\t"
      "s_cmp_ge_u32 s42, s45\n\t"
      "s_cselect_b32 s62, 5636096, s42\n\t"
      "s_add_u32 s42, s42, 1024\n\t"
      "buffer_load_dwordx4 v[112:115], v8, s[64:67], s62 offen sc1\n\t"
      "v_mfma_f32_32x32x16_bf16 v[80:95], v[244:247], v[116:119], v[80:95]\n\t"
      "s_cmp_eq_u32 s42, s44\n\t"
      "buffer_load_dwordx4 v[116:119], v9, s[64:67], s62 offen sc1\n\t"
      "s_cselect_b32 s42, s43, s42\n\t"
      "s_waitcnt vmcnt(13)\n\t"
      "v_mfma_f32_32x32x16_bf16 v[32:47], v[248:251], v[120:123], v[32:47]\n\t"
      "ds_read_b64_tr_b16 v[144:145], v4 offset:1024\n\t"
      "ds_read_b64_tr_b16 v[146:147], v4 offset:2048\n\t"
      "v_mfma_f32_32x32x16_bf16 v[48:63], v[252:255], v[120:123], v[48:63]\n\t"
      "ds_read_b64_tr_b16 v[148:149], v5 offset:1024\n\t"
      "ds_read_b64_tr_b16 v[150:151], v5 offset:2048\n\t"
      "s_waitcnt vmcnt(12)\n\t"
      "v_mfma_f32_32x32x16_bf16 v[64:79], v[248:251], v[124:127], v[64:79]\n\t"
      "s_cmp_ge_u32 s42, s45\n\t"
      "s_cselect_b32 s62, 5636096, s42\n\t"
      "s_add_u32 s42, s42, 1024\n\t"
      "buffer_load_dwordx4 v[120:123], v8, s[64:67], s62 offen sc1\n\t"
      "v_mfma_f32_32x32x16_bf16 v[80:95], v[252:255], v[124:127], v[80:95]\n\t"
      "s_cmp_eq_u32 s42, s44\n\t"
      "buffer_load_dwordx4 v[124:127], v9, s[64:67], s62 offen sc1\n\t"
      "s_cselect_b32 s42, s43, s42\n\t"
      "s_waitcnt lgkmcnt(0)\n\t"
      "s_waitcnt vmcnt(11)\n\t"
      "v_mfma_f32_32x32x16_bf16 v[32:47], v[144:147], v[96:99], v[32:47]\n\t"
      "ds_read_b64_tr_b16 v[152:153], v4 offset:3072\n\t"
      "ds_read_b64_tr_b16 v[154:155], v6 offset:12288\n\t"
      "v_mfma_f32_32x32x16_bf16 v[48:63], v[148:151], v[96:99], v[48:63]\n\t"
      "ds_read_b64_tr_b16 v[156:157], v5 offset:3072\n\t"
      "ds_read_b64_tr_b16 v[158:159], v7 offset:12288\n\t"
      "s_waitcnt vmcnt(10)\n\t"
      "v_mfma_f32_32x32x16_bf16 v[64:79], v[144:147], v[100:103], v[64:79]\n\t"
      "s_cmp_ge_u32 s42, s45\n\t"
      "s_cselect_b32 s62, 5636096, s42\n\t"
      "s_add_u32 s42, s42, 1024\n\t"
      "buffer_load_dwordx4 v[96:99], v8, s[64:67], s62 offen sc1\n\t"
      "v_mfma_f32_32x32x16_bf16 v[80:95], v[148:151], v[100:103], v[80:95]\n\t"
      "s_cmp_eq_u32 s42, s44\n\t"
      "buffer_load_dwordx4 v[100:103], v9, s[64:67], s62 offen sc1\n\t"
      "s_cselect_b32 s42, s43, s42\n\t"
      "s_waitcnt lgkmcnt(0)\n\t"
      "s_waitcnt vmcnt(9)\n\t"
      "v_mfma_f32_32x32x16_bf16 v[32:47], v[152:155], v[104:107], v[32:47]\n\t"
      "ds_read_b64_tr_b16 v[144:145], v6 offset:13312\n\t"
      "ds_read_b64_tr_b16 v[146:147], v6 offset:14336\n\t"
      "v_mfma_f32_32x32x16_bf16 v[48:63], v[156:159], v[104:107], v[48:63]\n\t"
      "ds_read_b64_tr_b16 v[148:149], v7 offset:13312\n\t"
      "ds_read_b64_tr_b16 v[150:151], v7 offset:14336\n\t"
      "s_waitcnt vmcnt(8)\n\t"
      "v_mfma_f32_32x32x16_bf16 v[64:79], v[152:155], v[108:111], v[64:79]\n\t"
      "s_cmp_ge_u32 s42, s45\n\t"
      "s_cselect_b32 s62, 5636096, s42\n\t"
      "s_add_u32 s42, s42, 1024\n\t"
      "buffer_load_dwordx4 v[104:107], v8, s[64:67], s62 offen sc1\n\t"
      "v_mfma_f32_32x32x16_bf16 v[80:95], v[156:159], v[108:111], v[80:95]\n\t"
      "s_cmp_eq_u32 s42, s44\n\t"
      "buffer_load_dwordx4 v[108:111], v9, s[64:67], s62 offen sc1\n\t"
      "s_cselect_b32 s42, s43, s42\n\t"
      "s_waitcnt lgkmcnt(0)\n\t"
      "s_waitcnt vmcnt(7)\n\t"
      "v_mfma_f32_32x32x16_bf16 v[32:47], v[144:147], v[112:115], v[32:47]\n\t"
      "ds_read_b64_tr_b16 v[152:153], v4 offset:4096\n\t"
      "ds_read_b64_tr_b16 v[154:155], v4 offset:5120\n\t"
      "v_mfma_f32_32x32x16_bf16 v[48:63], v[148:151], v[112:115], v[48:63]\n\t"
      "ds_read_b64_tr_b16 v[156:157], v5 offset:4096\n\t"
      "ds_read_b64_tr_b16 v[158:159], v5 offset:5120\n\t"
      "s_waitcnt vmcnt(6)\n\t"
      "v_mfma_f32_32x32x16_bf16 v[64:79], v[144:147], v[116:119], v[64:79]\n\t"
      "s_cmp_ge_u32 s42, s45\n\t"
      "s_cselect_b32 s62, 5636096, s42\n\t"
      "s_add_u32 s42, s42, 1024\n\t"
      "buffer_load_dwordx4 v[112:115], v8, s[64:67], s62 offen sc1\n\t"
      "v_mfma_f32_32x32x16_bf16 v[80:95], v[148:151], v[116:119], v[80:95]\n\t"
      "s_cmp_eq_u32 s42, s44\n\t"
      "buffer_load_dwordx4 v[116:119], v9, s[64:67], s62 offen sc1\n\t"
      "s_cselect_b32 s42, s43, s42\n\t"
      "s_waitcnt lgkmcnt(0)\n\t"
      "s_waitcnt vmcnt(7)\n\t"
      "v_mfma_f32_32x32x16_bf16 v[32:47], v[152:155], v[120:123], v[32:47]\n\t"
      "ds_read_b64_tr_b16 v[144:145], v4 offset:6144\n\t"
      "ds_read_b64_tr_b16 v[146:147], v4 offset:7168\n\t"
      "v_mfma_f32_32x32x16_bf16 v[48:63], v[156:159], v[120:123], v[48:63]\n\t"
      "ds_read_b64_tr_b16 v[148:149], v5 offset:6144\n\t"
      "ds_read_b64_tr_b16 v[150:151], v5 offset:7168\n\t"
      "s_waitcnt vmcnt(6)\n\t"
      "v_mfma_f32_32x32x16_bf16 v[64:79], v[152:155], v[124:127], v[64:79]\n\t"
      "s_cmp_ge_u32 s42, s45\n\t"
      "s_cselect_b32 s62, 5636096, s42\n\t"
      "s_add_u32 s42, s42, 1024\n\t"
      "buffer_load_dwordx4 v[120:123], v8, s[64:67], s62 offen sc1\n\t"
      "v_mfma_f32_32x32x16_bf16 v[80:95], v[156:159], v[124:127], v[80:95]\n\t"
      "s_cmp_eq_u32 s42, s44\n\t"
      "buffer_load_dwordx4 v[124:127], v9, s[64:67], s62 offen sc1\n\t"
      "s_cselect_b32 s42, s43, s42\n\t"
      "s_waitcnt lgkmcnt(0)\n\t"
      "s_waitcnt vmcnt(7)\n\t"
      "v_mfma_f32_32x32x16_bf16 v[32:47], v[144:147], v[96:99], v[32:47]\n\t"
      "ds_read_b64_tr_b16 v[152:153], v6 offset:24576\n\t"
      "ds_read_b64_tr_b16 v[154:155], v6 offset:25600\n\t"
      "v_mfma_f32_32x32x16_bf16 v[48:63], v[148:151], v[96:99], v[48:63]\n\t"
      "ds_read_b64_tr_b16 v[156:157], v7 offset:24576\n\t"
      "ds_read_b64_tr_b16 v[158:159], v7 offset:25600\n\t"
      "s_waitcnt vmcnt(6)\n\t"
      "v_mfma_f32_32x32x16_bf16 v[64:79], v[144:147], v[100:103], v[64:79]\n\t"
      "s_cmp_ge_u32 s42, s45\n\t"
      "s_cselect_b32 s62, 5636096, s42\n\t"
      "s_add_u32 s42, s42, 1024\n\t"
      "buffer_load_dwordx4 v[96:99], v8, s[64:67], s62 offen sc1\n\t"
      "v_mfma_f32_32x32x16_bf16 v[80:95], v[148:151], v[100:103], v[80:95]\n\t"
      "s_cmp_eq_u32 s42, s44\n\t"
      "buffer_load_dwordx4 v[100:103], v9, s[64:67], s62 offen sc1\n\t"
      "s_cselect_b32 s42, s43, s42\n\t"
      "s_waitcnt lgkmcnt(0)\n\t"
      "s_waitcnt vmcnt(7)\n\t"
      "v_mfma_f32_32x32x16_bf16 v[32:47], v[152:155], v[104:107], v[32:47]\n\t"
      "ds_read_b64_tr_b16 v[144:145], v6 offset:26624\n\t"
      "ds_read_b64_tr_b16 v[146:147], v6 offset:36864\n\t"
      "v_mfma_f32_32x32x16_bf16 v[48:63], v[156:159], v[104:107], v[48:63]\n\t"
      "ds_read_b64_tr_b16 v[148:149], v7 offset:26624\n\t"
      "ds_read_b64_tr_b16 v[150:151], v7 offset:36864\n\t"
      "s_waitcnt vmcnt(6)\n\t"
      "v_mfma_f32_32x32x16_bf16 v[64:79], v[152:155], v[108:111], v[64:79]\n\t"
      "s_cmp_ge_u32 s42, s45\n\t"
      "s_cselect_b32 s62, 5636096, s42\n\t"
      "s_add_u32 s42, s42, 1024\n\t"
      "buffer_load_dwordx4 v[104:107], v8, s[64:67], s62 offen sc1\n\t"
      "v_mfma_f32_32x32x16_bf16 v[80:95], v[156:159], v[108:111], v[80:95]\n\t"
      "s_cmp_eq_u32 s42, s44\n\t"
      "buffer_load_dwordx4 v[108:111], v9, s[64:67], s62 offen sc1\n\t"
      "s_cselect_b32 s42, s43, s42\n\t"
      "global_load_dwordx2 v[28:29], v18, s[86:87] sc1\n\t"
      "s_waitcnt lgkmcnt(0)\n\t"
      "s_waitcnt vmcnt(8)\n\t"
      "v_mfma_f32_32x32x16_bf16 v[32:47], v[144:147], v[112:115], v[32:47]\n\t"
      "ds_read_b64_tr_b16 v[152:153], v6 offset:37888\n\t"
      "ds_read_b64_tr_b16 v[154:155], v6 offset:38912\n\t"
      "v_mfma_f32_32x32x16_bf16 v[48:63], v[148:151], v[112:115], v[48:63]\n\t"
      "ds_read_b64_tr_b16 v[156:157], v7 offset:37888\n\t"
      "ds_read_b64_tr_b16 v[158:159], v7 offset:38912\n\t"
      "s_waitcnt vmcnt(7)\n\t"
      "v_mfma_f32_32x32x16_bf16 v[64:79], v[144:147], v[116:119], v[64:79]\n\t"
      "v_mfma_f32_32x32x16_bf16 v[80:95], v[148:151], v[116:119], v[80:95]\n\t"
      "s_waitcnt lgkmcnt(0)\n\t"
      "s_waitcnt vmcnt(6)\n\t"
      "v_mfma_f32_32x32x16_bf16 v[32:47], v[152:155], v[120:123], v[32:47]\n\t"
      "ds_read_b64_tr_b16 v[144:145], v6 offset:0\n\t"
      "ds_read_b64_tr_b16 v[146:147], v6 offset:1024\n\t"
      "v_mfma_f32_32x32x16_bf16 v[48:63], v[156:159], v[120:123], v[48:63]\n\t"
      "ds_read_b64_tr_b16 v[148:149], v7 offset:0\n\t"
      "ds_read_b64_tr_b16 v[150:151], v7 offset:1024\n\t"
      "s_waitcnt vmcnt(5)\n\t"
      "v_mfma_f32_32x32x16_bf16 v[64:79], v[152:155], v[124:127], v[64:79]\n\t"
      "v_mfma_f32_32x32x16_bf16 v[80:95], v[156:159], v[124:127], v[80:95]\n\t"
      "s_waitcnt lgkmcnt(0)\n\t"
      "s_waitcnt vmcnt(4)\n\t"
      "v_mfma_f32_32x32x16_bf16 v[32:47], v[144:147], v[96:99], v[32:47]\n\t"
      "ds_read_b64_tr_b16 v[152:153], v6 offset:2048\n\t"
      "ds_read_b64_tr_b16 v[154:155], v4 offset:0\n\t"
      "v_mfma_f32_32x32x16_bf16 v[48:63], v[148:151], v[96:99], v[48:63]\n\t"
      "ds_read_b64_tr_b16 v[156:157], v7 offset:2048\n\t"
      "ds_read_b64_tr_b16 v[158:159], v5 offset:0\n\t"
      "s_waitcnt vmcnt(3)\n\t"
      "v_mfma_f32_32x32x16_bf16 v[64:79], v[144:147], v[100:103], v[64:79]\n\t"
      "v_mfma_f32_32x32x16_bf16 v[80:95], v[148:151], v[100:103], v[80:95]\n\t"
      "s_waitcnt lgkmcnt(0)\n\t"
      "s_waitcnt vmcnt(2)\n\t"
      "v_mfma_f32_32x32x16_bf16 v[32:47], v[152:155], v[104:107], v[32:47]\n\t"
      "v_mfma_f32_32x32x16_bf16 v[48:63], v[156:159], v[104:107], v[48:63]\n\t"
      "s_waitcnt vmcnt(1)\n\t"
      "v_mfma_f32_32x32x16_bf16 v[64:79], v[152:155], v[108:111], v[64:79]\n\t"
      "s_waitcnt vmcnt(0)\n\t"
      "v_cmp_ne_u64 s[52:53], v[28:29], v[24:25]\n\t"
      "buffer_load_dwordx4 v[160:163], v19, s[68:71], 0 offen sc1\n\t"
      "s_mov_b32 s63, 65536\n\t"
      "buffer_load_dwordx4 v[164:167], v19, s[68:71], s63 offen sc1\n\t"
      "v_mfma_f32_32x32x16_bf16 v[80:95], v[156:159], v[108:111], v[80:95]\n\t"
      "s_nop 8\n\t"
      "ds_write_b128 v12, v[48:51] offset:0\n\t"
      "ds_write_b128 v12, v[52:55] offset:1024\n\t"
      "ds_write_b128 v12, v[56:59] offset:2048\n\t"
      "ds_write_b128 v12, v[60:63] offset:3072\n\t"
      "ds_write_b128 v12, v[64:67] offset:4096\n\t"
      "ds_write_b128 v12, v[68:71] offset:5120\n\t"
      "ds_write_b128 v12, v[72:75] offset:6144\n\t"
      "ds_write_b128 v12, v[76:79] offset:7168\n\t"
      "ds_write_b128 v13, v[80:83] offset:0\n\t"
      "ds_write_b128 v13, v[84:87] offset:1024\n\t"
      "ds_write_b128 v13, v[88:91] offset:2048\n\t"
      "ds_write_b128 v13, v[92:95] offset:12288\n\t"
      "s_cmp_eq_u64 s[52:53], 0\n\t"
      "s_cbranch_scc0 7f\n\t"
      "4:\n\t"
      "s_waitcnt vmcnt(0)\n\t"
      "ds_write_b128 v26, v[160:163] offset:36864\n\t"
      "ds_write_b128 v26, v[164:167] offset:37888\n\t"
      "ds_read_b64_tr_b16 v[168:169], v21 offset:36864\n\t"
      "ds_read_b64_tr_b16 v[170:171], v21 offset:37376\n\t"
      "ds_read_b64_tr_b16 v[172:173], v21 offset:37888\n\t"
      "ds_read_b64_tr_b16 v[174:175], v21 offset:38400\n\t"
      "v_lshlrev_b32 v28, 16, v31\n\t"
      "v_and_b32 v29, 0xffff0000, v31\n\t"
      "s_waitcnt lgkmcnt(0)\n\t"
      "s_barrier\n\t"
      "ds_read_b128 v[208:211], v14 offset:0\n\t"
      "ds_read_b128 v[212:215], v14 offset:1024\n\t"
      "ds_read_b128 v[216:219], v14 offset:2048\n\t"
      "ds_read_b128 v[220:223], v14 offset:3072\n\t"
      "ds_read_b128 v[224:227], v15 offset:4096\n\t"
      "ds_read_b128 v[228:231], v15 offset:5120\n\t"
      "ds_read_b128 v[232:235], v15 offset:6144\n\t"
      "ds_read_b128 v[236:239], v15 offset:7168\n\t"
      "ds_read_b128 v[240:243], v16 offset:0\n\t"
      "ds_read_b128 v[244:247], v16 offset:1024\n\t"
      "ds_read_b128 v[248:251], v16 offset:2048\n\t"
      "ds_read_b128 v[252:255], v16 offset:12288\n\t"
      "v_lshlrev_b32 v176, 16, v168\n\t"
      "v_and_b32 v177, 0xffff0000, v168\n\t"
      "v_pk_mul_f32 v[176:177], v[176:177], v[28:29] op_sel_hi:[1,0]\n\t"
      "v_lshlrev_b32 v178, 16, v169\n\t"
      "v_and_b32 v179, 0xffff0000, v169\n\t"
      "v_pk_mul_f32 v[178:179], v[178:179], v[28:29] op_sel_hi:[1,0]\n\t"
      "v_lshlrev_b32 v180, 16, v170\n\t"
      "v_and_b32 v181, 0xffff0000, v170\n\t"
      "v_pk_mul_f32 v[180:181], v[180:181], v[28:29] op_sel_hi:[1,0]\n\t"
      "v_lshlrev_b32 v182, 16, v171\n\t"
      "v_and_b32 v183, 0xffff0000, v171\n\t"
      "v_pk_mul_f32 v[182:183], v[182:183], v[28:29] op_sel_hi:[1,0]\n\t"
      "v_lshlrev_b32 v184, 16, v172\n\t"
      "v_and_b32 v185, 0xffff0000, v172\n\t"
      "v_pk_mul_f32 v[184:185], v[184:185], v[28:29] op_sel_hi:[1,0]\n\t"
      "v_lshlrev_b32 v186, 16, v173\n\t"
      "v_and_b32 v187, 0xffff0000, v173\n\t"
      "v_pk_mul_f32 v[186:187], v[186:187], v[28:29] op_sel_hi:[1,0]\n\t"
      "v_lshlrev_b32 v188, 16, v174\n\t"
      "v_and_b32 v189, 0xffff0000, v174\n\t"
      "v_pk_mul_f32 v[188:189], v[188:189], v[28:29] op_sel_hi:[1,0]\n\t"
      "v_lshlrev_b32 v190, 16, v175\n\t"
      "v_and_b32 v191, 0xffff0000, v175\n\t"
      "v_pk_mul_f32 v[190:191], v[190:191], v[28:29] op_sel_hi:[1,0]\n\t"
      "s_waitcnt lgkmcnt(8)\n\t"
      "v_pk_add_f32 v[32:33], v[32:33], v[208:209]\n\t"
      "v_pk_add_f32 v[34:35], v[34:35], v[210:211]\n\t"
      "v_pk_add_f32 v[36:37], v[36:37], v[212:213]\n\t"
      "v_pk_add_f32 v[38:39], v[38:39], v[214:215]\n\t"
      "v_pk_add_f32 v[40:41], v[40:41], v[216:217]\n\t"
      "v_pk_add_f32 v[42:43], v[42:43], v[218:219]\n\t"
      "v_pk_add_f32 v[44:45], v[44:45], v[220:221]\n\t"
      "v_pk_add_f32 v[46:47], v[46:47], v[222:223]\n\t"
      "s_waitcnt lgkmcnt(4)\n\t"
      "v_pk_add_f32 v[32:33], v[32:33], v[224:225]\n\t"
      "v_pk_add_f32 v[34:35], v[34:35], v[226:227]\n\t"
      "v_pk_add_f32 v[36:37], v[36:37], v[228:229]\n\t"
      "v_pk_add_f32 v[38:39], v[38:39], v[230:231]\n\t"
      "v_pk_add_f32 v[40:41], v[40:41], v[232:233]\n\t"
      "v_pk_add_f32 v[42:43], v[42:43], v[234:235]\n\t"
      "v_pk_add_f32 v[44:45], v[44:45], v[236:237]\n\t"
      "v_pk_add_f32 v[46:47], v[46:47], v[238:239]\n\t"
      "s_waitcnt lgkmcnt(2)\n\t"
      "v_pk_add_f32 v[32:33], v[32:33], v[240:241]\n\t"
      "v_pk_add_f32 v[34:35], v[34:35], v[242:243]\n\t"
      "v_pk_add_f32 v[36:37], v[36:37], v[244:245]\n\t"
      "v_pk_add_f32 v[38:39], v[38:39], v[246:247]\n\t"
      "v_pk_fma_f32 v[32:33], v[32:33], v[28:29], v[176:177] op_sel:[0,1,0] op_sel_hi:[1,1,1]\n\t"
      "v_pk_fma_f32 v[34:35], v[34:35], v[28:29], v[178:179] op_sel:[0,1,0] op_sel_hi:[1,1,1]\n\t"
      "v_pk_fma_f32 v[36:37], v[36:37], v[28:29], v[180:181] op_sel:[0,1,0] op_sel_hi:[1,1,1]\n\t"
      "v_pk_fma_f32 v[38:39], v[38:39], v[28:29], v[182:183] op_sel:[0,1,0] op_sel_hi:[1,1,1]\n\t"
      "v_cvt_pk_bf16_f32 v192, v32, v33\n\t"
      "v_cvt_pk_bf16_f32 v194, v36, v37\n\t"
      "v_cvt_pk_bf16_f32 v193, v34, v35\n\t"
      "v_cvt_pk_bf16_f32 v195, v38, v39\n\t"
      "s_nop 0\n\t"
      "v_permlane32_swap_b32 v192, v194\n\t"
      "v_permlane32_swap_b32 v193, v195\n\t"
      "s_nop 0\n\t"
      "buffer_store_dwordx4 v[192:195], v22, s[24:27], 0 offen offset:0\n\t"
      "s_waitcnt lgkmcnt(0)\n\t"
      "v_pk_add_f32 v[40:41], v[40:41], v[248:249]\n\t"
      "v_pk_add_f32 v[42:43], v[42:43], v[250:251]\n\t"
      "v_pk_add_f32 v[44:45], v[44:45], v[252:253]\n\t"
      "v_pk_add_f32 v[46:47], v[46:47], v[254:255]\n\t"
      "v_pk_fma_f32 v[40:41], v[40:41], v[28:29], v[184:185] op_sel:[0,1,0] op_sel_hi:[1,1,1]\n\t"
      "v_pk_fma_f32 v[42:43], v[42:43], v[28:29], v[186:187] op_sel:[0,1,0] op_sel_hi:[1,1,1]\n\t"
      "v_pk_fma_f32 v[44:45], v[44:45], v[28:29], v[188:189] op_sel:[0,1,0] op_sel_hi:[1,1,1]\n\t"
      "v_pk_fma_f32 v[46:47], v[46:47], v[28:29], v[190:191] op_sel:[0,1,0] op_sel_hi:[1,1,1]\n\t"
      "v_cvt_pk_bf16_f32 v196, v40, v41\n\t"
      "v_cvt_pk_bf16_f32 v198, v44, v45\n\t"
      "v_cvt_pk_bf16_f32 v197, v42, v43\n\t"
      "v_cvt_pk_bf16_f32 v199, v46, v47\n\t"
      "s_nop 0\n\t"
      "v_permlane32_swap_b32 v196, v198\n\t"
      "v_permlane32_swap_b32 v197, v199\n\t"
      "s_nop 0\n\t"
      "buffer_store_dwordx4 v[196:199], v22, s[24:27], 0 offen offset:32\n\t"
      "s_endpgm\n\t"
      "30:\n\t"
      "v_lshlrev_b32 v17, 4, v1\n\t"
      "s_lshl2_add_u32 s57, s80, s84\n\t"
      "s_lshl_b32 s40, s57, 15\n\t"
      "global_load_dwordx4 v[80:83], v17, s[76:77]\n\t"
      "buffer_load_dwordx4 v[96:99], v17, s[24:27], s40 offen\n\t"
      "buffer_load_dwordx4 v[100:103], v17, s[24:27], s40 offen offset:1024\n\t"
      "buffer_load_dwordx4 v[104:107], v17, s[24:27], s40 offen offset:2048\n\t"
      "buffer_load_dwordx4 v[108:111], v17, s[24:27], s40 offen offset:3072\n\t"
      "s_add_u32 s40, s40, 4096\n\t"
      "buffer_load_dwordx4 v[112:115], v17, s[24:27], s40 offen\n\t"
      "buffer_load_dwordx4 v[116:119], v17, s[24:27], s40 offen offset:1024\n\t"
      "buffer_load_dwordx4 v[120:123], v17, s[24:27], s40 offen offset:2048\n\t"
      "buffer_load_dwordx4 v[124:127], v17, s[24:27], s40 offen offset:3072\n\t"
      "s_lshl_b32 s57, s84, 2\n\t"
      "s_add_u32 s57, s57, 81888\n\t"
      "v_mov_b32 v180, 81888\n\t"
      "v_mov_b32 v181, s57\n\t"
      "v_mov_b32 v182, 0\n\t"
      "ds_write_b32 v181, v182\n\t"
      "s_waitcnt lgkmcnt(0)\n\t"
      "s_barrier\n\t"
      "s_lshl_b32 s57, s84, 11\n\t"
      "s_add_u32 s57, s57, 34816\n\t"
      "v_lshlrev_b32 v18, 4, v1\n\t"
      "v_add_u32 v18, s57, v18\n\t"
      "v_lshrrev_b32 v28, 4, v1\n\t"
      "v_bfe_u32 v29, v1, 2, 2\n\t"
      "v_lshl_add_u32 v19, v28, 2, v29\n\t"
      "v_and_b32 v27, 3, v1\n\t"
      "v_lshlrev_b32 v19, 7, v19\n\t"
      "v_lshl_add_u32 v19, v27, 3, v19\n\t"
      "s_lshl_b32 s57, s84, 5\n\t"
      "s_add_u32 s57, s57, 34816\n\t"
      "v_add_u32 v19, s57, v19\n\t"
      "s_mul_i32 s57, s84, 2304\n\t"
      "s_add_u32 s57, s57, 43008\n\t"
      "s_movk_i32 s62, 144\n\t"
      "v_and_b32 v20, 15, v1\n\t"
      "v_mul_u32_u24 v20, s62, v20\n\t"
      "v_lshl_add_u32 v20, v28, 3, v20\n\t"
      "v_add_u32 v20, s57, v20\n\t"
      "v_lshrrev_b32 v22, 3, v1\n\t"
      "v_and_b32 v26, 7, v1\n\t"
      "v_mul_u32_u24 v21, s62, v22\n\t"
      "v_lshl_add_u32 v21, v26, 4, v21\n\t"
      "v_add_u32 v21, s57, v21\n\t"
      "v_and_b32 v11, 1, v28\n\t"
      "v_lshlrev_b32 v11, 2, v11\n\t"
      "v_xor_b32 v11, v26, v11\n\t"
      "v_lshlrev_b32 v11, 4, v11\n\t"
      "v_lshl_add_u32 v11, v22, 10, v11\n\t"
      "v_lshlrev_b32 v22, 12, v22\n\t"
      "v_lshl_add_u32 v22, v26, 4, v22\n\t"
      "s_lshl2_add_u32 s57, s95, s84\n\t"
      "s_lshl_b32 s62, s57, 5\n\t"
      "s_add_u32 s62, s62, s85\n\t"
      "s_lshl_b32 s62, s62, 3\n\t"
      "s_add_u32 s62, s62, 8192\n\t"
      "v_mov_b32 v23, s62\n\t"
      "s_lshl_b32 s57, s57, 16\n\t"
      "s_lshl_b32 s62, s85, 7\n\t"
      "s_add_u32 s85, s57, s62\n\t"
      "s_mul_i32 s89, s84, 360448\n\t"
      "s_lshl_b32 s62, s83, 7\n\t"
      "s_add_u32 s89, s89, s62\n\t"
      "s_add_u32 s90, s89, 360448\n\t"
      "s_and_b32 s95, s82, 3\n\t"
      "s_mul_i32 s95, s95, 11\n\t"
      "s_lshr_b32 s95, s95, 1\n\t"
      "s_lshl_b32 s62, s95, 14\n\t"
      "s_add_u32 s56, s89, s62\n\t"
      "s_mul_i32 s91, s84, 12288\n\t"
      "s_waitcnt vmcnt(10)\n\t"
      "ds_write_b128 v18, v[164:167]\n\t"
      "v_mov_b32 v182, 1\n\t"
      "s_waitcnt vmcnt(9)\n\t"
      "ds_write_b128 v18, v[168:171] offset:1024\n\t"
      "ds_write_b32 v181, v182\n\t"
      "s_mov_b32 s88, 0x400000\n\t"
      "50:\n\t"
      "ds_read_b128 v[184:187], v180\n\t"
      "s_waitcnt lgkmcnt(0)\n\t"
      "v_min_u32 v184, v184, v185\n\t"
      "v_min3_u32 v184, v184, v186, v187\n\t"
      "v_cmp_gt_u32 vcc, 1, v184\n\t"
      "s_sub_u32 s88, s88, 1\n\t"
      "s_cbranch_scc1 51f\n\t"
      "s_nop 2\n\t"
      "s_cbranch_vccnz 50b\n\t"
      "51:\n\t"
      "ds_read_b64_tr_b16 v[172:173], v19 offset:0\n\t"
      "ds_read_b64_tr_b16 v[174:175], v19 offset:2048\n\t"
      "ds_read_b64_tr_b16 v[176:177], v19 offset:4096\n\t"
      "ds_read_b64_tr_b16 v[178:179], v19 offset:6144\n\t"
      "s_waitcnt lgkmcnt(3)\n\t"
      "ds_write_b64 v20, v[172:173] offset:0\n\t"
      "s_waitcnt lgkmcnt(3)\n\t"
      "ds_write_b64 v20, v[174:175] offset:32\n\t"
      "s_waitcnt lgkmcnt(3)\n\t"
      "ds_write_b64 v20, v[176:177] offset:64\n\t"
      "s_waitcnt lgkmcnt(3)\n\t"
      "ds_write_b64 v20, v[178:179] offset:96\n\t"
      "ds_read_b128 v[164:167], v21\n\t"
      "ds_read_b128 v[168:171], v21 offset:1152\n\t"
      "v_add_co_u32 v24, vcc, 1, v24\n\t"
      "s_nop 1\n\t"
      "v_addc_co_u32 v25, vcc, 0, v25, vcc\n\t"
      "v_readfirstlane_b32 s94, v24\n\t"
      "s_nop 0\n\t"
      "s_and_b32 s94, s94, 1\n\t"
      "s_lshl_b32 s94, s94, 24\n\t"
      "s_add_u32 s86, s72, s94\n\t"
      "s_addc_u32 s87, s73, 0\n\t"
      "s_add_u32 s68, s86, 1048576\n\t"
      "s_addc_u32 s69, s87, 0\n\t"
      "s_and_b32 s69, s69, 0xffff\n\t"
      "s_mov_b32 s70, 2097152\n\t"
      "s_mov_b32 s71, 0x20000\n\t"
      "s_add_u32 s63, s85, 32768\n\t"
      "s_waitcnt lgkmcnt(1)\n\t"
      "buffer_store_dwordx4 v[164:167], v22, s[68:71], s85 offen sc1\n\t"
      "s_waitcnt lgkmcnt(0)\n\t"
      "buffer_store_dwordx4 v[168:171], v22, s[68:71], s63 offen sc1\n\t"
      "s_add_u32 s40, s40, 4096\n\t"
      "buffer_load_dwordx4 v[128:131], v17, s[24:27], s40 offen\n\t"
      "buffer_load_dwordx4 v[132:135], v17, s[24:27], s40 offen offset:1024\n\t"
      "buffer_load_dwordx4 v[136:139], v17, s[24:27], s40 offen offset:2048\n\t"
      "buffer_load_dwordx4 v[140:143], v17, s[24:27], s40 offen offset:3072\n\t"
      "s_add_u32 s40, s40, 4096\n\t"
      "buffer_load_dwordx4 v[144:147], v17, s[24:27], s40 offen\n\t"
      "buffer_load_dwordx4 v[148:151], v17, s[24:27], s40 offen offset:1024\n\t"
      "buffer_load_dwordx4 v[152:155], v17, s[24:27], s40 offen offset:2048\n\t"
      "buffer_load_dwordx4 v[156:159], v17, s[24:27], s40 offen offset:3072\n\t"
      "s_add_u32 s40, s40, 4096\n\t"
      "buffer_load_dwordx4 v[192:195], v17, s[24:27], s40 offen\n\t"
      "buffer_load_dwordx4 v[196:199], v17, s[24:27], s40 offen offset:1024\n\t"
      "buffer_load_dwordx4 v[200:203], v17, s[24:27], s40 offen offset:2048\n\t"
      "buffer_load_dwordx4 v[204:207], v17, s[24:27], s40 offen offset:3072\n\t"
      "s_add_u32 s40, s40, 4096\n\t"
      "buffer_load_dwordx4 v[208:211], v17, s[24:27], s40 offen\n\t"
      "buffer_load_dwordx4 v[212:215], v17, s[24:27], s40 offen offset:1024\n\t"
      "buffer_load_dwordx4 v[216:219], v17, s[24:27], s40 offen offset:2048\n\t"
      "buffer_load_dwordx4 v[220:223], v17, s[24:27], s40 offen offset:3072\n\t"
      "s_add_u32 s40, s40, 4096\n\t"
      "buffer_load_dwordx4 v[224:227], v17, s[24:27], s40 offen\n\t"
      "buffer_load_dwordx4 v[228:231], v17, s[24:27], s40 offen offset:1024\n\t"
      "buffer_load_dwordx4 v[232:235], v17, s[24:27], s40 offen offset:2048\n\t"
      "buffer_load_dwordx4 v[236:239], v17, s[24:27], s40 offen offset:3072\n\t"
      "s_add_u32 s40, s40, 4096\n\t"
      "buffer_load_dwordx4 v[240:243], v17, s[24:27], s40 offen\n\t"
      "buffer_load_dwordx4 v[244:247], v17, s[24:27], s40 offen offset:1024\n\t"
      "buffer_load_dwordx4 v[248:251], v17, s[24:27], s40 offen offset:2048\n\t"
      "buffer_load_dwordx4 v[252:255], v17, s[24:27], s40 offen offset:3072\n\t"
      "s_mul_i32 s57, s84, 8704\n\t"
      "s_add_u32 s57, s57, 81920\n\t"
      "v_lshlrev_b32 v28, 2, v1\n\t"
      "v_add_u32 v28, s57, v28\n\t"
      "v_mul_u32_u24 v29, 272, v2\n\t"
      "v_lshl_add_u32 v29, v3, 7, v29\n\t"
      "v_add_u32 v29, s57, v29\n\t"
      "v_mov_b32 v32, 0\n\t"
      "v_mov_b32 v33, 0\n\t"
      "v_mov_b32 v34, 0\n\t"
      "v_mov_b32 v35, 0\n\t"
      "v_mov_b32 v36, 0\n\t"
      "v_mov_b32 v37, 0\n\t"
      "v_mov_b32 v38, 0\n\t"
      "v_mov_b32 v39, 0\n\t"
      "v_mov_b32 v40, 0\n\t"
      "v_mov_b32 v41, 0\n\t"
      "v_mov_b32 v42, 0\n\t"
      "v_mov_b32 v43, 0\n\t"
      "v_mov_b32 v44, 0\n\t"
      "v_mov_b32 v45, 0\n\t"
      "v_mov_b32 v46, 0\n\t"
      "v_mov_b32 v47, 0\n\t"
      "v_mov_b32 v48, 0\n\t"
      "v_mov_b32 v49, 0\n\t"
      "v_mov_b32 v50, 0\n\t"
      "v_mov_b32 v51, 0\n\t"
      "v_mov_b32 v52, 0\n\t"
      "v_mov_b32 v53, 0\n\t"
      "v_mov_b32 v54, 0\n\t"
      "v_mov_b32 v55, 0\n\t"
      "v_mov_b32 v56, 0\n\t"
      "v_mov_b32 v57, 0\n\t"
      "v_mov_b32 v58, 0\n\t"
      "v_mov_b32 v59, 0\n\t"
      "v_mov_b32 v60, 0\n\t"
      "v_mov_b32 v61, 0\n\t"
      "v_mov_b32 v62, 0\n\t"
      "v_mov_b32 v63, 0\n\t"
      "s_waitcnt vmcnt(30)\n\t"
      "v_dot2c_f32_bf16 v32, v96, v80\n\t"
      "v_dot2c_f32_bf16 v33, v100, v80\n\t"
      "v_dot2c_f32_bf16 v34, v104, v80\n\t"
      "v_dot2c_f32_bf16 v35, v108, v80\n\t"
      "v_dot2c_f32_bf16 v32, v97, v81\n\t"
      "v_dot2c_f32_bf16 v33, v101, v81\n\t"
      "v_dot2c_f32_bf16 v34, v105, v81\n\t"
      "v_dot2c_f32_bf16 v35, v109, v81\n\t"
      "v_dot2c_f32_bf16 v32, v98, v82\n\t"
      "v_dot2c_f32_bf16 v33, v102, v82\n\t"
      "v_dot2c_f32_bf16 v34, v106, v82\n\t"
      "v_dot2c_f32_bf16 v35, v110, v82\n\t"
      "v_dot2c_f32_bf16 v32, v99, v83\n\t"
      "v_dot2c_f32_bf16 v33, v103, v83\n\t"
      "v_dot2c_f32_bf16 v34, v107, v83\n\t"
      "v_dot2c_f32_bf16 v35, v111, v83\n\t"
      "s_waitcnt vmcnt(26)\n\t"
      "v_dot2c_f32_bf16 v36, v112, v80\n\t"
      "v_dot2c_f32_bf16 v37, v116, v80\n\t"
      "v_dot2c_f32_bf16 v38, v120, v80\n\t"
      "v_dot2c_f32_bf16 v39, v124, v80\n\t"
      "v_dot2c_f32_bf16 v36, v113, v81\n\t"
      "v_dot2c_f32_bf16 v37, v117, v81\n\t"
      "v_dot2c_f32_bf16 v38, v121, v81\n\t"
      "v_dot2c_f32_bf16 v39, v125, v81\n\t"
      "v_dot2c_f32_bf16 v36, v114, v82\n\t"
      "v_dot2c_f32_bf16 v37, v118, v82\n\t"
      "v_dot2c_f32_bf16 v38, v122, v82\n\t"
      "v_dot2c_f32_bf16 v39, v126, v82\n\t"
      "v_dot2c_f32_bf16 v36, v115, v83\n\t"
      "v_dot2c_f32_bf16 v37, v119, v83\n\t"
      "v_dot2c_f32_bf16 v38, v123, v83\n\t"
      "v_dot2c_f32_bf16 v39, v127, v83\n\t"
      "s_waitcnt vmcnt(20)\n\t"
      "s_mov_b64 s[58:59], exec\n\t"
      "s_mov_b64 exec, 1\n\t"
      "s_nop 0\n\t"
      "global_store_dwordx2 v23, v[24:25], s[86:87] sc1\n\t"
      "s_mov_b64 exec, s[58:59]\n\t"
      "v_dot2c_f32_bf16 v40, v128, v80\n\t"
      "v_dot2c_f32_bf16 v41, v132, v80\n\t"
      "v_dot2c_f32_bf16 v42, v136, v80\n\t"
      "v_dot2c_f32_bf16 v43, v140, v80\n\t"
      "v_dot2c_f32_bf16 v40, v129, v81\n\t"
      "v_dot2c_f32_bf16 v41, v133, v81\n\t"
      "v_dot2c_f32_bf16 v42, v137, v81\n\t"
      "v_dot2c_f32_bf16 v43, v141, v81\n\t"
      "v_dot2c_f32_bf16 v40, v130, v82\n\t"
      "v_dot2c_f32_bf16 v41, v134, v82\n\t"
      "v_dot2c_f32_bf16 v42, v138, v82\n\t"
      "v_dot2c_f32_bf16 v43, v142, v82\n\t"
      "v_dot2c_f32_bf16 v40, v131, v83\n\t"
      "v_dot2c_f32_bf16 v41, v135, v83\n\t"
      "v_dot2c_f32_bf16 v42, v139, v83\n\t"
      "v_dot2c_f32_bf16 v43, v143, v83\n\t"
      "s_waitcnt vmcnt(17)\n\t"
      "v_dot2c_f32_bf16 v44, v144, v80\n\t"
      "v_dot2c_f32_bf16 v45, v148, v80\n\t"
      "v_dot2c_f32_bf16 v46, v152, v80\n\t"
      "v_dot2c_f32_bf16 v47, v156, v80\n\t"
      "v_dot2c_f32_bf16 v44, v145, v81\n\t"
      "v_dot2c_f32_bf16 v45, v149, v81\n\t"
      "v_dot2c_f32_bf16 v46, v153, v81\n\t"
      "v_dot2c_f32_bf16 v47, v157, v81\n\t"
      "v_dot2c_f32_bf16 v44, v146, v82\n\t"
      "v_dot2c_f32_bf16 v45, v150, v82\n\t"
      "v_dot2c_f32_bf16 v46, v154, v82\n\t"
      "v_dot2c_f32_bf16 v47, v158, v82\n\t"
      "v_dot2c_f32_bf16 v44, v147, v83\n\t"
      "v_dot2c_f32_bf16 v45, v151, v83\n\t"
      "v_dot2c_f32_bf16 v46, v155, v83\n\t"
      "v_dot2c_f32_bf16 v47, v159, v83\n\t"
      "s_waitcnt vmcnt(13)\n\t"
      "v_dot2c_f32_bf16 v48, v192, v80\n\t"
      "v_dot2c_f32_bf16 v49, v196, v80\n\t"
      "v_dot2c_f32_bf16 v50, v200, v80\n\t"
      "v_dot2c_f32_bf16 v51, v204, v80\n\t"
      "v_dot2c_f32_bf16 v48, v193, v81\n\t"
      "v_dot2c_f32_bf16 v49, v197, v81\n\t"
      "v_dot2c_f32_bf16 v50, v201, v81\n\t"
      "v_dot2c_f32_bf16 v51, v205, v81\n\t"
      "v_dot2c_f32_bf16 v48, v194, v82\n\t"
      "v_dot2c_f32_bf16 v49, v198, v82\n\t"
      "v_dot2c_f32_bf16 v50, v202, v82\n\t"
      "v_dot2c_f32_bf16 v51, v206, v82\n\t"
      "v_dot2c_f32_bf16 v48, v195, v83\n\t"
      "v_dot2c_f32_bf16 v49, v199, v83\n\t"
      "v_dot2c_f32_bf16 v50, v203, v83\n\t"
      "v_dot2c_f32_bf16 v51, v207, v83\n\t"
      "s_waitcnt vmcnt(9)\n\t"
      "v_dot2c_f32_bf16 v52, v208, v80\n\t"
      "v_dot2c_f32_bf16 v53, v212, v80\n\t"
      "v_dot2c_f32_bf16 v54, v216, v80\n\t"
      "v_dot2c_f32_bf16 v55, v220, v80\n\t"
      "v_dot2c_f32_bf16 v52, v209, v81\n\t"
      "v_dot2c_f32_bf16 v53, v213, v81\n\t"
      "v_dot2c_f32_bf16 v54, v217, v81\n\t"
      "v_dot2c_f32_bf16 v55, v221, v81\n\t"
      "v_dot2c_f32_bf16 v52, v210, v82\n\t"
      "v_dot2c_f32_bf16 v53, v214, v82\n\t"
      "v_dot2c_f32_bf16 v54, v218, v82\n\t"
      "v_dot2c_f32_bf16 v55, v222, v82\n\t"
      "v_dot2c_f32_bf16 v52, v211, v83\n\t"
      "v_dot2c_f32_bf16 v53, v215, v83\n\t"
      "v_dot2c_f32_bf16 v54, v219, v83\n\t"
      "v_dot2c_f32_bf16 v55, v223, v83\n\t"
      "v_mfma_f32_32x32x16_bf16 v[164:179], v[80:83], v[80:83], v[164:179]\n\t"
      "v_mfma_f32_32x32x16_bf16 v[164:179], v[80:83], v[80:83], v[164:179]\n\t"
      "v_mfma_f32_32x32x16_bf16 v[164:179], v[80:83], v[80:83], v[164:179]\n\t"
      "v_mfma_f32_32x32x16_bf16 v[164:179], v[80:83], v[80:83], v[164:179]\n\t"
      "v_mfma_f32_32x32x16_bf16 v[164:179], v[80:83], v[80:83], v[164:179]\n\t"
      "v_mfma_f32_32x32x16_bf16 v[164:179], v[80:83], v[80:83], v[164:179]\n\t"
      "v_mfma_f32_32x32x16_bf16 v[164:179], v[80:83], v[80:83], v[164:179]\n\t"
      "v_mfma_f32_32x32x16_bf16 v[164:179], v[80:83], v[80:83], v[164:179]\n\t"
      "s_barrier\n\t"
      "s_waitcnt vmcnt(5)\n\t"
      "v_dot2c_f32_bf16 v56, v224, v80\n\t"
      "v_dot2c_f32_bf16 v57, v228, v80\n\t"
      "v_dot2c_f32_bf16 v58, v232, v80\n\t"
      "v_dot2c_f32_bf16 v59, v236, v80\n\t"
      "v_dot2c_f32_bf16 v56, v225, v81\n\t"
      "v_dot2c_f32_bf16 v57, v229, v81\n\t"
      "v_dot2c_f32_bf16 v58, v233, v81\n\t"
      "v_dot2c_f32_bf16 v59, v237, v81\n\t"
      "v_dot2c_f32_bf16 v56, v226, v82\n\t"
      "v_dot2c_f32_bf16 v57, v230, v82\n\t"
      "v_dot2c_f32_bf16 v58, v234, v82\n\t"
      "v_dot2c_f32_bf16 v59, v238, v82\n\t"
      "v_dot2c_f32_bf16 v56, v227, v83\n\t"
      "v_dot2c_f32_bf16 v57, v231, v83\n\t"
      "v_dot2c_f32_bf16 v58, v235, v83\n\t"
      "v_dot2c_f32_bf16 v59, v239, v83\n\t"
      "s_waitcnt vmcnt(1)\n\t"
      "v_dot2c_f32_bf16 v60, v240, v80\n\t"
      "v_dot2c_f32_bf16 v61, v244, v80\n\t"
      "v_dot2c_f32_bf16 v62, v248, v80\n\t"
      "v_dot2c_f32_bf16 v63, v252, v80\n\t"
      "v_dot2c_f32_bf16 v60, v241, v81\n\t"
      "v_dot2c_f32_bf16 v61, v245, v81\n\t"
      "v_dot2c_f32_bf16 v62, v249, v81\n\t"
      "v_dot2c_f32_bf16 v63, v253, v81\n\t"
      "v_dot2c_f32_bf16 v60, v242, v82\n\t"
      "v_dot2c_f32_bf16 v61, v246, v82\n\t"
      "v_dot2c_f32_bf16 v62, v250, v82\n\t"
      "v_dot2c_f32_bf16 v63, v254, v82\n\t"
      "v_dot2c_f32_bf16 v60, v243, v83\n\t"
      "v_dot2c_f32_bf16 v61, v247, v83\n\t"
      "v_dot2c_f32_bf16 v62, v251, v83\n\t"
      "v_dot2c_f32_bf16 v63, v255, v83\n\t"
      "s_nop 2\n\t"
      "ds_write_b32 v28, v32 offset:0\n\t"
      "ds_write_b32 v28, v33 offset:272\n\t"
      "ds_write_b32 v28, v34 offset:544\n\t"
      "ds_write_b32 v28, v35 offset:816\n\t"
      "ds_write_b32 v28, v36 offset:1088\n\t"
      "ds_write_b32 v28, v37 offset:1360\n\t"
      "ds_write_b32 v28, v38 offset:1632\n\t"
      "ds_write_b32 v28, v39 offset:1904\n\t"
      "ds_write_b32 v28, v40 offset:2176\n\t"
      "ds_write_b32 v28, v41 offset:2448\n\t"
      "ds_write_b32 v28, v42 offset:2720\n\t"
      "ds_write_b32 v28, v43 offset:2992\n\t"
      "ds_write_b32 v28, v44 offset:3264\n\t"
      "ds_write_b32 v28, v45 offset:3536\n\t"
      "ds_write_b32 v28, v46 offset:3808\n\t"
      "ds_write_b32 v28, v47 offset:4080\n\t"
      "ds_write_b32 v28, v48 offset:4352\n\t"
      "ds_write_b32 v28, v49 offset:4624\n\t"
      "ds_write_b32 v28, v50 offset:4896\n\t"
      "ds_write_b32 v28, v51 offset:5168\n\t"
      "ds_write_b32 v28, v52 offset:5440\n\t"
      "ds_write_b32 v28, v53 offset:5712\n\t"
      "ds_write_b32 v28, v54 offset:5984\n\t"
      "ds_write_b32 v28, v55 offset:6256\n\t"
      "ds_write_b32 v28, v56 offset:6528\n\t"
      "ds_write_b32 v28, v57 offset:6800\n\t"
      "ds_write_b32 v28, v58 offset:7072\n\t"
      "ds_write_b32 v28, v59 offset:7344\n\t"
      "ds_write_b32 v28, v60 offset:7616\n\t"
      "ds_write_b32 v28, v61 offset:7888\n\t"
      "ds_write_b32 v28, v62 offset:8160\n\t"
      "ds_write_b32 v28, v63 offset:8432\n\t"
      "ds_read_b128 v[64:67], v29 offset:0\n\t"
      "ds_read_b128 v[68:71], v29 offset:16\n\t"
      "ds_read_b128 v[72:75], v29 offset:32\n\t"
      "ds_read_b128 v[76:79], v29 offset:48\n\t"
      "ds_read_b128 v[80:83], v29 offset:64\n\t"
      "ds_read_b128 v[84:87], v29 offset:80\n\t"
      "ds_read_b128 v[88:91], v29 offset:96\n\t"
      "ds_read_b128 v[92:95], v29 offset:112\n\t"
      "s_add_u32 s64, s86, 5242880\n\t"
      "s_addc_u32 s65, s87, 0\n\t"
      "s_and_b32 s65, s65, 0xffff\n\t"
      "s_mov_b32 s66, 5636096\n\t"
      "s_mov_b32 s67, 0x20000\n\t"
      "s_lshl2_add_u32 s62, s80, s84\n\t"
      "s_mul_i32 s92, s62, 86\n\t"
      "s_mul_i32 s63, s81, 6\n\t"
      "s_add_u32 s92, s92, s63\n\t"
      "s_lshl_b32 s92, s92, 10\n\t"
      "s_lshl_b32 s62, s62, 4\n\t"
      "s_add_u32 s62, s62, s81\n\t"
      "s_lshl_b32 s62, s62, 3\n\t"
      "s_add_u32 s62, s62, 16384\n\t"
      "v_mov_b32 v23, s62\n\t"
      "v_lshlrev_b32 v17, 4, v1\n\t"
      "s_add_u32 s36, s86, 32768\n\t"
      "s_addc_u32 s37, s87, 0\n\t"
      "s_and_b32 s37, s37, 0xffff\n\t"
      "s_mov_b32 s38, 8192\n\t"
      "s_mov_b32 s39, 0x20000\n\t"
      "s_waitcnt lgkmcnt(0)\n\t"
      "v_mfma_f32_32x32x16_bf16 v[96:111], v[80:83], v[80:83], v[96:111]\n\t"
      "v_mfma_f32_32x32x16_bf16 v[96:111], v[80:83], v[80:83], v[96:111]\n\t"
      "v_mfma_f32_32x32x16_bf16 v[96:111], v[80:83], v[80:83], v[96:111]\n\t"
      "v_mfma_f32_32x32x16_bf16 v[96:111], v[80:83], v[80:83], v[96:111]\n\t"
      "v_mfma_f32_32x32x16_bf16 v[96:111], v[80:83], v[80:83], v[96:111]\n\t"
      "v_mfma_f32_32x32x16_bf16 v[96:111], v[80:83], v[80:83], v[96:111]\n\t"
      "v_mfma_f32_32x32x16_bf16 v[96:111], v[80:83], v[80:83], v[96:111]\n\t"
      "v_mfma_f32_32x32x16_bf16 v[96:111], v[80:83], v[80:83], v[96:111]\n\t"
      "s_barrier\n\t"
      "v_pk_add_f32 v[64:65], v[64:65], v[80:81]\n\t"
      "v_pk_add_f32 v[66:67], v[66:67], v[82:83]\n\t"
      "v_pk_add_f32 v[68:69], v[68:69], v[84:85]\n\t"
      "v_pk_add_f32 v[70:71], v[70:71], v[86:87]\n\t"
      "v_pk_add_f32 v[72:73], v[72:73], v[88:89]\n\t"
      "v_pk_add_f32 v[74:75], v[74:75], v[90:91]\n\t"
      "v_pk_add_f32 v[76:77], v[76:77], v[92:93]\n\t"
      "v_pk_add_f32 v[78:79], v[78:79], v[94:95]\n\t"
      "v_pk_add_f32 v[64:65], v[64:65], v[72:73]\n\t"
      "v_pk_add_f32 v[66:67], v[66:67], v[74:75]\n\t"
      "v_pk_add_f32 v[68:69], v[68:69], v[76:77]\n\t"
      "v_pk_add_f32 v[70:71], v[70:71], v[78:79]\n\t"
      "v_pk_add_f32 v[64:65], v[64:65], v[68:69]\n\t"
      "v_pk_add_f32 v[66:67], v[66:67], v[70:71]\n\t"
      "v_pk_add_f32 v[64:65], v[64:65], v[66:67]\n\t"
      "v_add_f32 v64, v64, v65\n\t"
      "s_nop 0\n\t"
      "v_mov_b32 v65, v64\n\t"
      "s_nop 1\n\t"
      "v_permlane32_swap_b32 v64, v65\n\t"
      "v_add_f32 v64, v64, v65\n\t"
      "v_cvt_pk_bf16_f32 v64, v64, v64\n\t"
      "v_lshlrev_b32 v64, 16, v64\n\t"
      "v_mul_f32 v64, 0xbfb8aa3b, v64\n\t"
      "v_exp_f32 v64, v64\n\t"
      "s_nop 0\n\t"
      "v_add_f32 v64, 1.0, v64\n\t"
      "v_rcp_f32 v64, v64\n\t"
      "s_nop 0\n\t"
      "v_cvt_pk_bf16_f32 v64, v64, v64\n\t"
      "v_lshlrev_b32 v64, 16, v64\n\t"
      "v_sub_f32 v65, 1.0, v64\n\t"
      "v_cvt_pk_bf16_f32 v64, v64, v65\n\t"
      "s_lshl2_add_u32 s57, s80, s84\n\t"
      "s_lshl_b32 s57, s57, 7\n\t"
      "v_lshlrev_b32 v65, 2, v2\n\t"
      "buffer_store_dword v64, v65, s[36:39], s57 offen sc1\n\t"
      "v_bfe_u32 v28, v1, 2, 2\n\t"
      "v_lshl_add_u32 v29, v3, 2, v28\n\t"
      "v_lshlrev_b32 v20, 7, v29\n\t"
      "v_bfe_u32 v27, v1, 4, 1\n\t"
      "v_bfe_u32 v21, v1, 1, 1\n\t"
      "v_lshl_add_u32 v21, v27, 1, v21\n\t"
      "v_lshrrev_b32 v28, 1, v28\n\t"
      "v_lshlrev_b32 v28, 2, v28\n\t"
      "v_xor_b32 v21, v21, v28\n\t"
      "v_lshl_add_u32 v20, v21, 4, v20\n\t"
      "v_and_b32 v28, 1, v1\n\t"
      "v_lshl_add_u32 v20, v28, 3, v20\n\t"
      "s_and_b32 s62, s84, 1\n\t"
      "s_lshl_b32 s62, s62, 6\n\t"
      "v_xor_b32 v20, s62, v20\n\t"
      "v_add_u32 v18, s91, v20\n\t"
      "v_xor_b32 v19, 64, v18\n\t"
      "v_mfma_f32_32x32x16_bf16 v[96:111], v[80:83], v[80:83], v[96:111]\n\t"
      "v_mfma_f32_32x32x16_bf16 v[96:111], v[80:83], v[80:83], v[96:111]\n\t"
      "v_mfma_f32_32x32x16_bf16 v[96:111], v[80:83], v[80:83], v[96:111]\n\t"
      "v_mfma_f32_32x32x16_bf16 v[96:111], v[80:83], v[80:83], v[96:111]\n\t"
      "v_mfma_f32_32x32x16_bf16 v[96:111], v[80:83], v[80:83], v[96:111]\n\t"
      "v_mfma_f32_32x32x16_bf16 v[96:111], v[80:83], v[80:83], v[96:111]\n\t"
      "v_mfma_f32_32x32x16_bf16 v[96:111], v[80:83], v[80:83], v[96:111]\n\t"
      "v_mfma_f32_32x32x16_bf16 v[96:111], v[80:83], v[80:83], v[96:111]\n\t"
      "s_barrier\n\t"
      "s_waitcnt vmcnt(0)\n\t"
      "s_mov_b64 s[58:59], exec\n\t"
      "s_mov_b64 exec, 1\n\t"
      "s_nop 0\n\t"
      "global_store_dwordx2 v23, v[24:25], s[86:87] sc1\n\t"
      "s_mov_b64 exec, s[58:59]\n\t"
      "s_add_u32 m0, s61, 0\n\t"
      "s_add_u32 s57, s56, 8192\n\t"
      "buffer_load_dwordx4 v11, s[32:35], s56 offen lds\n\t"
      "s_cmp_eq_u32 s57, s90\n\t"
      "s_cselect_b32 s56, s89, s57\n\t"
      "s_add_u32 m0, s61, 1024\n\t"
      "s_add_u32 s57, s56, 8192\n\t"
      "buffer_load_dwordx4 v11, s[32:35], s56 offen lds\n\t"
      "s_cmp_eq_u32 s57, s90\n\t"
      "s_cselect_b32 s56, s89, s57\n\t"
      "s_add_u32 m0, s61, 2048\n\t"
      "s_add_u32 s57, s56, 8192\n\t"
      "buffer_load_dwordx4 v11, s[32:35], s56 offen lds\n\t"
      "s_cmp_eq_u32 s57, s90\n\t"
      "s_cselect_b32 s56, s89, s57\n\t"
      "s_add_u32 m0, s60, 0\n\t"
      "s_add_u32 s57, s56, 8192\n\t"
      "buffer_load_dwordx4 v11, s[32:35], s56 offen lds\n\t"
      "s_cmp_eq_u32 s57, s90\n\t"
      "s_cselect_b32 s56, s89, s57\n\t"
      "s_add_u32 m0, s60, 1024\n\t"
      "s_add_u32 s57, s56, 8192\n\t"
      "buffer_load_dwordx4 v11, s[32:35], s56 offen lds\n\t"
      "s_cmp_eq_u32 s57, s90\n\t"
      "s_cselect_b32 s56, s89, s57\n\t"
      "s_add_u32 m0, s60, 2048\n\t"
      "s_add_u32 s57, s56, 8192\n\t"
      "buffer_load_dwordx4 v11, s[32:35], s56 offen lds\n\t"
      "s_cmp_eq_u32 s57, s90\n\t"
      "s_cselect_b32 s56, s89, s57\n\t"
      "s_add_u32 m0, s60, 3072\n\t"
      "s_add_u32 s57, s56, 8192\n\t"
      "buffer_load_dwordx4 v11, s[32:35], s56 offen lds\n\t"
      "s_cmp_eq_u32 s57, s90\n\t"
      "s_cselect_b32 s56, s89, s57\n\t"
      "s_add_u32 m0, s61, 12288\n\t"
      "s_add_u32 s57, s56, 8192\n\t"
      "buffer_load_dwordx4 v11, s[32:35], s56 offen lds\n\t"
      "s_cmp_eq_u32 s57, s90\n\t"
      "s_cselect_b32 s56, s89, s57\n\t"
      "s_add_u32 m0, s61, 13312\n\t"
      "s_add_u32 s57, s56, 8192\n\t"
      "buffer_load_dwordx4 v11, s[32:35], s56 offen lds\n\t"
      "s_cmp_eq_u32 s57, s90\n\t"
      "s_cselect_b32 s56, s89, s57\n\t"
      "s_add_u32 m0, s61, 14336\n\t"
      "s_add_u32 s57, s56, 8192\n\t"
      "buffer_load_dwordx4 v11, s[32:35], s56 offen lds\n\t"
      "s_cmp_eq_u32 s57, s90\n\t"
      "s_cselect_b32 s56, s89, s57\n\t"
      "s_add_u32 m0, s60, 4096\n\t"
      "s_add_u32 s57, s56, 8192\n\t"
      "buffer_load_dwordx4 v11, s[32:35], s56 offen lds\n\t"
      "s_cmp_eq_u32 s57, s90\n\t"
      "s_cselect_b32 s56, s89, s57\n\t"
      "s_add_u32 m0, s60, 5120\n\t"
      "s_add_u32 s57, s56, 8192\n\t"
      "buffer_load_dwordx4 v11, s[32:35], s56 offen lds\n\t"
      "s_cmp_eq_u32 s57, s90\n\t"
      "s_cselect_b32 s56, s89, s57\n\t"
      "s_add_u32 m0, s60, 6144\n\t"
      "s_add_u32 s57, s56, 8192\n\t"
      "buffer_load_dwordx4 v11, s[32:35], s56 offen lds\n\t"
      "s_cmp_eq_u32 s57, s90\n\t"
      "s_cselect_b32 s56, s89, s57\n\t"
      "s_add_u32 m0, s60, 7168\n\t"
      "s_add_u32 s57, s56, 8192\n\t"
      "buffer_load_dwordx4 v11, s[32:35], s56 offen lds\n\t"
      "s_cmp_eq_u32 s57, s90\n\t"
      "s_cselect_b32 s56, s89, s57\n\t"
      "s_waitcnt lgkmcnt(0)\n\t"
      "v_xor_b32 v5, 64, v20\n\t"
      "v_add_u32 v6, s61, v20\n\t"
      "v_add_u32 v7, s61, v5\n\t"
      "v_add_u32 v4, s60, v20\n\t"
      "v_add_u32 v5, s60, v5\n\t"
      "s_and_b32 s48, s84, 1\n\t"
      "v_lshlrev_b32 v21, 6, v29\n\t"
      "v_lshl_add_u32 v21, v27, 5, v21\n\t"
      "v_and_b32 v28, 3, v1\n\t"
      "v_lshl_add_u32 v21, v28, 3, v21\n\t"
      "s_lshr_b32 s49, s84, 1\n\t"
      "s_mul_i32 s50, s49, 88064\n\t"
      "v_add_u32 v8, s50, v17\n\t"
      "s_sub_u32 s50, 88064, s50\n\t"
      "v_add_u32 v9, s50, v17\n\t"
      "s_lshl1_add_u32 s40, s83, s48\n\t"
      "s_lshl_b32 s40, s40, 5\n\t"
      "s_lshl1_add_u32 s41, s82, s49\n\t"
      "s_lshl_b32 s41, s41, 5\n\t"
      "v_lshrrev_b32 v19, 2, v1\n\t"
      "v_add_u32 v19, s40, v19\n\t"
      "v_lshlrev_b32 v19, 12, v19\n\t"
      "v_lshl_add_u32 v19, v28, 4, v19\n\t"
      "s_lshl_b32 s50, s41, 1\n\t"
      "v_add_u32 v19, s50, v19\n\t"
      "v_add_u32 v29, s40, v2\n\t"
      "s_lshr_b32 s50, s82, 4\n\t"
      "s_lshl_b32 s50, s50, 12\n\t"
      "s_add_u32 s50, s50, 24576\n\t"
      "v_lshlrev_b32 v18, 3, v29\n\t"
      "v_add_u32 v18, s50, v18\n\t"
      "v_lshlrev_b32 v26, 1, v29\n\t"
      "v_xor_b32 v20, 64, v26\n\t"
      "v_add_u32 v29, s41, v2\n\t"
      "v_lshlrev_b32 v31, 2, v29\n\t"
      "v_lshlrev_b32 v22, 10, v29\n\t"
      "v_lshl_add_u32 v22, v3, 4, v22\n\t"
      "s_lshl_b32 s50, s40, 1\n\t"
      "v_add_u32 v22, s50, v22\n\t"
      "s_mul_i32 s48, s84, 11\n\t"
      "s_mul_i32 s49, s48, 43\n\t"
      "s_lshr_b32 s49, s49, 7\n\t"
      "s_add_u32 s48, s48, 10\n\t"
      "s_mul_i32 s48, s48, 43\n\t"
      "s_lshr_b32 s48, s48, 7\n\t"
      "s_min_u32 s48, s48, 14\n\t"
      "v_and_b32 v28, 15, v1\n\t"
      "v_max_u32 v29, s49, v28\n\t"
      "v_min_u32 v29, s48, v29\n\t"
      "v_cmp_eq_u32 vcc, 15, v28\n\t"
      "s_lshl_b32 s48, s82, 8\n\t"
      "s_add_u32 s48, s48, 16384\n\t"
      "v_cndmask_b32 v29, v29, v28, vcc\n\t"
      "v_bfe_u32 v28, v1, 4, 1\n\t"
      "v_lshl_add_u32 v29, v28, 4, v29\n\t"
      "v_lshlrev_b32 v10, 3, v29\n\t"
      "v_add_u32 v10, s48, v10\n\t"
      "s_mul_i32 s43, s82, 172\n\t"
      "s_mul_i32 s48, s84, 22\n\t"
      "s_add_u32 s43, s43, s48\n\t"
      "s_lshl_b32 s43, s43, 10\n\t"
      "s_add_u32 s44, s43, 22528\n\t"
      "s_cmp_eq_u32 s84, 3\n\t"
      "s_cselect_b32 s48, 2048, 0\n\t"
      "s_sub_u32 s45, s44, s48\n\t"
      "s_lshl_b32 s48, s95, 10\n\t"
      "s_add_u32 s42, s43, s48\n\t"
      "s_mov_b32 s36, s74\n\t"
      "s_and_b32 s37, s75, 0xffff\n\t"
      "s_mov_b32 s38, 1024\n\t"
      "s_mov_b32 s39, 0x20000\n\t"
      "s_add_u32 s68, s86, 3145728\n\t"
      "s_addc_u32 s69, s87, 0\n\t"
      "s_and_b32 s69, s69, 0xffff\n\t"
      "s_mov_b32 s70, 2097152\n\t"
      "s_mov_b32 s71, 0x20000\n\t"
      "s_add_u32 s28, s86, 32768\n\t"
      "s_addc_u32 s29, s87, 0\n\t"
      "s_and_b32 s29, s29, 0xffff\n\t"
      "s_mov_b32 s30, 8192\n\t"
      "s_mov_b32 s31, 0x20000\n\t"
      "s_mov_b32 s24, s78\n\t"
      "s_and_b32 s25, s79, 0xffff\n\t"
      "s_mov_b32 s26, 2097152\n\t"
      "s_mov_b32 s27, 0x20000\n\t"
      "v_mov_b32 v28, 0x3e80\n\t"
      "v_cmp_eq_u32 vcc, 0, v3\n\t"
      "v_mov_b32 v149, 0\n\t"
      "v_mov_b32 v150, 0\n\t"
      "v_mov_b32 v151, 0\n\t"
      "v_mov_b32 v153, 0\n\t"
      "v_mov_b32 v154, 0\n\t"
      "v_mov_b32 v155, 0\n\t"
      "v_mov_b32 v157, 0\n\t"
      "v_mov_b32 v158, 0\n\t"
      "v_mov_b32 v159, 0\n\t"
      "v_cndmask_b32 v156, 0, v28, vcc\n\t"
      "v_mfma_f32_32x32x16_bf16 v[96:111], v[80:83], v[80:83], v[96:111]\n\t"
      "v_mfma_f32_32x32x16_bf16 v[96:111], v[80:83], v[80:83], v[96:111]\n\t"
      "v_mfma_f32_32x32x16_bf16 v[96:111], v[80:83], v[80:83], v[96:111]\n\t"
      "v_mfma_f32_32x32x16_bf16 v[96:111], v[80:83], v[80:83], v[96:111]\n\t"
      "v_mfma_f32_32x32x16_bf16 v[96:111], v[80:83], v[80:83], v[96:111]\n\t"
      "v_mfma_f32_32x32x16_bf16 v[96:111], v[80:83], v[80:83], v[96:111]\n\t"
      "v_mfma_f32_32x32x16_bf16 v[96:111], v[80:83], v[80:83], v[96:111]\n\t"
      "v_mfma_f32_32x32x16_bf16 v[96:111], v[80:83], v[80:83], v[96:111]\n\t"
      "s_barrier\n\t"
      "v_mfma_f32_32x32x16_bf16 v[96:111], v[80:83], v[80:83], v[96:111]\n\t"
      "v_mfma_f32_32x32x16_bf16 v[96:111], v[80:83], v[80:83], v[96:111]\n\t"
      "v_mfma_f32_32x32x16_bf16 v[96:111], v[80:83], v[80:83], v[96:111]\n\t"
      "v_mfma_f32_32x32x16_bf16 v[96:111], v[80:83], v[80:83], v[96:111]\n\t"
      "v_mfma_f32_32x32x16_bf16 v[96:111], v[80:83], v[80:83], v[96:111]\n\t"
      "v_mfma_f32_32x32x16_bf16 v[96:111], v[80:83], v[80:83], v[96:111]\n\t"
      "v_mfma_f32_32x32x16_bf16 v[96:111], v[80:83], v[80:83], v[96:111]\n\t"
      "v_mfma_f32_32x32x16_bf16 v[96:111], v[80:83], v[80:83], v[96:111]\n\t"
      "s_barrier\n\t"
      "v_mfma_f32_32x32x16_bf16 v[96:111], v[80:83], v[80:83], v[96:111]\n\t"
      "v_mfma_f32_32x32x16_bf16 v[96:111], v[80:83], v[80:83], v[96:111]\n\t"
      "v_mfma_f32_32x32x16_bf16 v[96:111], v[80:83], v[80:83], v[96:111]\n\t"
      "v_mfma_f32_32x32x16_bf16 v[96:111], v[80:83], v[80:83], v[96:111]\n\t"
      "v_mfma_f32_32x32x16_bf16 v[96:111], v[80:83], v[80:83], v[96:111]\n\t"
      "v_mfma_f32_32x32x16_bf16 v[96:111], v[80:83], v[80:83], v[96:111]\n\t"
      "v_mfma_f32_32x32x16_bf16 v[96:111], v[80:83], v[80:83], v[96:111]\n\t"
      "v_mfma_f32_32x32x16_bf16 v[96:111], v[80:83], v[80:83], v[96:111]\n\t"
      "s_barrier\n\t"
      "s_nop 11\n\t"
      "s_waitcnt vmcnt(0)\n\t"
      "s_waitcnt vmcnt(0)\n\t"
      "s_barrier\n\t"
      "s_sleep 8\n\t"
      "s_branch 31b\n\t"
      "7:\n\t"
      "s_mov_b32 s88, 200000\n\t"
      "3:\n\t"
      "global_load_dwordx2 v[168:169], v18, s[86:87] sc1\n\t"
      "s_waitcnt vmcnt(0)\n\t"
      "v_cmp_ne_u64 vcc, v[168:169], v[24:25]\n\t"
      "s_nop 4\n\t"
      "s_cbranch_vccz 5f\n\t"
      "s_sub_u32 s88, s88, 1\n\t"
      "s_cbranch_scc1 5f\n\t"
      "s_sleep 4\n\t"
      "s_branch 3b\n\t"
      "5:\n\t"
      "buffer_load_dwordx4 v[160:163], v19, s[68:71], 0 offen sc1\n\t"
      "buffer_load_dwordx4 v[164:167], v19, s[68:71], s63 offen sc1\n\t"
      "s_branch 4b\n\t"
      "201:\n\t"
      "s_mov_b32 s52, s42\n\t"
      "s_and_b32 s53, s43, 0xffff\n\t"
      "s_mov_b32 s54, 1409024\n\t"
      "s_mov_b32 s55, 0x20000\n\t"
      "s_mul_i32 s61, s56, 43\n\t"
      "v_add_u32 v46, s61, v1\n\t"
      "v_lshrrev_b32 v47, 2, v46\n\t"
      "v_and_b32 v46, 3, v46\n\t"
      "s_lshr_b32 s61, %[bid], 3\n\t"
      "s_mul_i32 s61, s61, 44032\n\t"
      "s_and_b32 s62, %[bid], 1\n\t"
      "s_lshl_b32 s62, s62, 9\n\t"
      "s_add_u32 s61, s61, s62\n\t"
      "v_lshlrev_b32 v47, 10, v47\n\t"
      "v_lshl_add_u32 v46, v46, 7, v47\n\t"
      "v_add_u32 v46, s61, v46\n\t"
      "s_mov_b32 s74, -1\n\t"
      "s_mov_b32 s75, 0x7ff\n\t"
      "s_setprio 0\n\t"
      "s_lshl_b32 s61, s56, 2\n\t"
      "s_xor_b32 s61, s61, 4\n\t"
      "s_add_u32 s62, s61, 81856\n\t"
      "v_mov_b32 v20, s62\n\t"
      "s_mul_i32 s72, s56, 8704\n\t"
      "s_mul_i32 s73, s58, 16640\n\t"
      "s_add_u32 s73, s73, 34816\n\t"
      "s_lshr_b32 s61, s60, 4\n\t"
      "s_lshl_b32 s61, s61, 8\n\t"
      "s_add_u32 s61, s61, 8192\n\t"
      "v_lshlrev_b32 v12, 3, v2\n\t"
      "v_add_u32 v12, s61, v12\n\t"
      "v_lshlrev_b32 v32, 4, v1\n\t"
      "s_lshl_b32 s61, s57, 11\n\t"
      "v_add_u32 v13, s61, v32\n\t"
      "s_add_u32 s62, s73, 32\n\t"
      "v_add_u32 v14, s62, v13\n\t"
      "v_lshlrev_b32 v15, 5, v2\n\t"
      "v_lshl_add_u32 v15, v3, 4, v15\n\t"
      "v_add_u32 v15, s72, v15\n\t"
      "v_cmp_eq_u32 vcc, 0, v3\n\t"
      "v_lshlrev_b32 v22, 4, v2\n\t"
      "v_lshlrev_b32 v25, 3, v2\n\t"
      "v_add_u32 v16, 1024, v22\n\t"
      "v_add_u32 v17, 1536, v25\n\t"
      "v_cndmask_b32 v16, v17, v16, vcc\n\t"
      "v_add_u32 v22, 1032, v22\n\t"
      "v_add_u32 v25, 1792, v25\n\t"
      "v_cndmask_b32 v17, v25, v22, vcc\n\t"
      "v_add_u32 v16, s72, v16\n\t"
      "v_add_u32 v17, s72, v17\n\t"
      "v_lshlrev_b32 v18, 2, v2\n\t"
      "v_lshl_add_u32 v18, v3, 9, v18\n\t"
      "s_add_u32 s61, s72, 2048\n\t"
      "v_add_u32 v18, s61, v18\n\t"
      "v_and_b32 v22, 15, v1\n\t"
      "v_lshrrev_b32 v25, 4, v1\n\t"
      "s_mul_i32 s61, s58, 4576\n\t"
      "s_add_u32 s61, s61, 68096\n\t"
      "s_lshl_b32 s62, s57, 8\n\t"
      "v_lshlrev_b32 v31, 5, v22\n\t"
      "v_lshlrev_b32 v28, 4, v25\n\t"
      "v_sub_u32 v28, v28, v31\n\t"
      "s_sub_u32 s63, s61, s62\n\t"
      "s_add_u32 s63, s63, 736\n\t"
      "v_add_u32 v28, s63, v28\n\t"
      "s_add_u32 s61, s61, 4080\n\t"
      "v_sub_u32 v33, s61, v32\n\t"
      "v_lshl_add_u32 v31, v25, 3, v31\n\t"
      "s_lshl_b32 s63, s57, 11\n\t"
      "s_add_u32 s63, s63, s73\n\t"
      "s_add_u32 s63, s63, 32\n\t"
      "v_add_u32 v30, s63, v31\n\t"
      "v_add_u32 v31, s72, v31\n\t"
      "v_add_u32 v22, 1, v22\n\t"
      "v_and_b32 v29, 3, v22\n\t"
      "v_mul_u32_u24 v29, 4160, v29\n\t"
      "v_lshrrev_b32 v22, 2, v22\n\t"
      "v_lshl_add_u32 v29, v22, 3, v29\n\t"
      "v_lshl_add_u32 v29, v25, 4, v29\n\t"
      "s_sub_u32 s63, s73, s62\n\t"
      "s_add_u32 s63, s63, 256\n\t"
      "v_add_u32 v29, s63, v29\n\t"
      "s_xor_b32 s61, s56, 1\n\t"
      "s_mul_i32 s61, s61, 8704\n\t"
      "v_add_u32 v34, s61, v32\n\t"
      "v_add_u32 v35, s72, v32\n\t"
      "s_waitcnt vmcnt(8)\n\t"
      "v_add_co_u32 v10, vcc, 1, v10\n\t"
      "s_nop 1\n\t"
      "v_addc_co_u32 v11, vcc, 0, v11, vcc\n\t"
      "s_lshl_b32 s61, s60, 12\n\t"
      "v_readfirstlane_b32 s65, v10\n\t"
      "s_nop 0\n\t"
      "s_and_b32 s65, s65, 1\n\t"
      "s_lshl_b32 s65, s65, 24\n\t"
      "s_add_u32 s76, s40, s65\n\t"
      "s_addc_u32 s77, s41, 0\n\t"
      "s_add_u32 s62, s61, 1048576\n\t"
      "s_add_u32 s44, s76, s62\n\t"
      "s_addc_u32 s45, s77, 0\n\t"
      "s_and_b32 s45, s45, 0xffff\n\t"
      "s_mov_b32 s46, 4096\n\t"
      "s_mov_b32 s47, 0x20000\n\t"
      "s_add_u32 s62, s61, 3145728\n\t"
      "s_add_u32 s48, s76, s62\n\t"
      "s_addc_u32 s49, s77, 0\n\t"
      "s_and_b32 s49, s49, 0xffff\n\t"
      "s_mov_b32 s50, 4096\n\t"
      "s_mov_b32 s51, 0x20000\n\t"
      "s_waitcnt vmcnt(0)\n\t"
      "v_lshlrev_b32 v42, 16, v42\n\t"
      "s_mov_b32 s61, 0x3fb8aa3b\n\t"
      "v_lshl_or_b32 v4, v37, 16, v36\n\t"
      "v_mul_f32 v22, s61, v42\n\t"
      "v_rndne_f32 v24, v22\n\t"
      "v_fma_f32 v25, v42, s61, -v22\n\t"
      "v_sub_f32 v22, v22, v24\n\t"
      "v_fmamk_f32 v25, v42, 0x32a5705f, v25\n\t"
      "v_add_f32 v22, v22, v25\n\t"
      "v_exp_f32 v22, v22\n\t"
      "v_cvt_i32_f32 v24, v24\n\t"
      "v_lshl_or_b32 v5, v39, 16, v38\n\t"
      "v_ldexp_f32 v22, v22, v24\n\t"
      "v_lshl_or_b32 v6, v41, 16, v40\n\t"
      "v_cvt_pk_bf16_f32 v22, v22, v22\n\t"
      "v_lshlrev_b32 v8, 16, v8\n\t"
      "v_lshlrev_b32 v7, 16, v22\n\t"
      "v_lshlrev_b32 v200, 16, v4\n\t"
      "v_and_b32 v201, 0xffff0000, v4\n\t"
      "v_mul_f32 v200, v7, v200\n\t"
      "v_mul_f32 v201, v7, v201\n\t"
      "v_mul_f32 v200, 0x3fb8aa3b, v200\n\t"
      "v_mul_f32 v201, 0.15915494, v201\n\t"
      "v_mul_f32 v212, 0x41000000, v200\n\t"
      "v_mul_f32 v213, 0x41000000, v201\n\t"
      "v_exp_f32 v212, v212\n\t"
      "v_fract_f32 v213, v213\n\t"
      "v_cos_f32 v206, v213\n\t"
      "v_sin_f32 v207, v213\n\t"
      "v_mul_f32 v214, 0x42000000, v200\n\t"
      "v_mul_f32 v215, 0x42000000, v201\n\t"
      "v_exp_f32 v214, v214\n\t"
      "v_fract_f32 v215, v215\n\t"
      "v_cos_f32 v208, v215\n\t"
      "v_sin_f32 v209, v215\n\t"
      "v_exp_f32 v216, v200\n\t"
      "v_fract_f32 v217, v201\n\t"
      "v_cos_f32 v204, v217\n\t"
      "v_sin_f32 v205, v217\n\t"
      "v_mul_f32 v218, 0x43800000, v200\n\t"
      "v_mul_f32 v219, 0x43800000, v201\n\t"
      "v_exp_f32 v218, v218\n\t"
      "v_fract_f32 v219, v219\n\t"
      "v_cos_f32 v210, v219\n\t"
      "v_sin_f32 v211, v219\n\t"
      "v_lshlrev_b32 v238, 16, v5\n\t"
      "v_and_b32 v239, 0xffff0000, v5\n\t"
      "v_pk_mul_f32 v[206:207], v[212:213], v[206:207] op_sel_hi:[0,1]\n\t"
      "v_pk_mul_f32 v[208:209], v[214:215], v[208:209] op_sel_hi:[0,1]\n\t"
      "v_pk_mul_f32 v[204:205], v[216:217], v[204:205] op_sel_hi:[0,1]\n\t"
      "v_pk_mul_f32 v[210:211], v[218:219], v[210:211] op_sel_hi:[0,1]\n\t"
      "v_mul_f32 v238, v7, v238\n\t"
      "v_mul_f32 v239, v7, v239\n\t"
      "v_lshlrev_b32 v240, 16, v6\n\t"
      "v_and_b32 v241, 0xffff0000, v6\n\t"
      "v_pk_mul_f32 v[220:221], v[240:241], v[238:239] op_sel_hi:[0,1]\n\t"
      "v_pk_fma_f32 v[202:203], v[240:241], v[238:239], v[220:221] op_sel:[1,1,0] op_sel_hi:[1,0,1] neg_lo:[1,0,0]\n\t"
      "v_mov_b32 v100, 0x3f80\n\t"
      "v_cvt_pk_bf16_f32 v101, v206, v207\n\t"
      "v_pk_mul_f32 v[220:221], v[206:207], v[206:207] op_sel_hi:[0,1]\n\t"
      "v_pk_fma_f32 v[216:217], v[206:207], v[206:207], v[220:221] op_sel:[1,1,0] op_sel_hi:[1,0,1] neg_lo:[1,0,0]\n\t"
      "v_cvt_pk_bf16_f32 v102, v216, v217\n\t"
      "v_pk_mul_f32 v[222:223], v[216:217], v[206:207] op_sel_hi:[0,1]\n\t"
      "v_pk_fma_f32 v[218:219], v[216:217], v[206:207], v[222:223] op_sel:[1,1,0] op_sel_hi:[1,0,1] neg_lo:[1,0,0]\n\t"
      "v_cvt_pk_bf16_f32 v103, v218, v219\n\t"
      "v_cvt_pk_bf16_f32 v104, v208, v209\n\t"
      "v_pk_mul_f32 v[220:221], v[206:207], v[208:209] op_sel_hi:[0,1]\n\t"
      "v_pk_fma_f32 v[214:215], v[206:207], v[208:209], v[220:221] op_sel:[1,1,0] op_sel_hi:[1,0,1] neg_lo:[1,0,0]\n\t"
      "v_cvt_pk_bf16_f32 v105, v214, v215\n\t"
      "v_pk_mul_f32 v[222:223], v[216:217], v[208:209] op_sel_hi:[0,1]\n\t"
      "v_pk_fma_f32 v[216:217], v[216:217], v[208:209], v[222:223] op_sel:[1,1,0] op_sel_hi:[1,0,1] neg_lo:[1,0,0]\n\t"
      "v_cvt_pk_bf16_f32 v106, v216, v217\n\t"
      "v_pk_mul_f32 v[220:221], v[218:219], v[208:209] op_sel_hi:[0,1]\n\t"
      "v_pk_fma_f32 v[218:219], v[218:219], v[208:209], v[220:221] op_sel:[1,1,0] op_sel_hi:[1,0,1] neg_lo:[1,0,0]\n\t"
      "v_cvt_pk_bf16_f32 v107, v218, v219\n\t"
      "v_pk_mul_f32 v[222:223], v[208:209], v[208:209] op_sel_hi:[0,1]\n\t"
      "v_pk_fma_f32 v[212:213], v[208:209], v[208:209], v[222:223] op_sel:[1,1,0] op_sel_hi:[1,0,1] neg_lo:[1,0,0]\n\t"
      "v_cvt_pk_bf16_f32 v108, v212, v213\n\t"
      "v_pk_mul_f32 v[222:223], v[214:215], v[208:209] op_sel_hi:[0,1]\n\t"
      "v_pk_fma_f32 v[214:215], v[214:215], v[208:209], v[222:223] op_sel:[1,1,0] op_sel_hi:[1,0,1] neg_lo:[1,0,0]\n\t"
      "v_cvt_pk_bf16_f32 v109, v214, v215\n\t"
      "v_pk_mul_f32 v[220:221], v[216:217], v[208:209] op_sel_hi:[0,1]\n\t"
      "v_pk_fma_f32 v[216:217], v[216:217], v[208:209], v[220:221] op_sel:[1,1,0] op_sel_hi:[1,0,1] neg_lo:[1,0,0]\n\t"
      "v_cvt_pk_bf16_f32 v110, v216, v217\n\t"
      "v_pk_mul_f32 v[222:223], v[218:219], v[208:209] op_sel_hi:[0,1]\n\t"
      "v_pk_fma_f32 v[218:219], v[218:219], v[208:209], v[222:223] op_sel:[1,1,0] op_sel_hi:[1,0,1] neg_lo:[1,0,0]\n\t"
      "v_cvt_pk_bf16_f32 v111, v218, v219\n\t"
      "v_pk_mul_f32 v[220:221], v[212:213], v[208:209] op_sel_hi:[0,1]\n\t"
      "v_pk_fma_f32 v[212:213], v[212:213], v[208:209], v[220:221] op_sel:[1,1,0] op_sel_hi:[1,0,1] neg_lo:[1,0,0]\n\t"
      "v_cvt_pk_bf16_f32 v112, v212, v213\n\t"
      "v_pk_mul_f32 v[222:223], v[214:215], v[208:209] op_sel_hi:[0,1]\n\t"
      "v_pk_fma_f32 v[214:215], v[214:215], v[208:209], v[222:223] op_sel:[1,1,0] op_sel_hi:[1,0,1] neg_lo:[1,0,0]\n\t"
      "v_cvt_pk_bf16_f32 v113, v214, v215\n\t"
      "v_pk_mul_f32 v[220:221], v[216:217], v[208:209] op_sel_hi:[0,1]\n\t"
      "v_pk_fma_f32 v[216:217], v[216:217], v[208:209], v[220:221] op_sel:[1,1,0] op_sel_hi:[1,0,1] neg_lo:[1,0,0]\n\t"
      "v_cvt_pk_bf16_f32 v114, v216, v217\n\t"
      "v_pk_mul_f32 v[222:223], v[218:219], v[208:209] op_sel_hi:[0,1]\n\t"
      "v_pk_fma_f32 v[218:219], v[218:219], v[208:209], v[222:223] op_sel:[1,1,0] op_sel_hi:[1,0,1] neg_lo:[1,0,0]\n\t"
      "v_cvt_pk_bf16_f32 v115, v218, v219\n\t"
      "v_pk_mul_f32 v[220:221], v[212:213], v[208:209] op_sel_hi:[0,1]\n\t"
      "v_pk_fma_f32 v[212:213], v[212:213], v[208:209], v[220:221] op_sel:[1,1,0] op_sel_hi:[1,0,1] neg_lo:[1,0,0]\n\t"
      "v_cvt_pk_bf16_f32 v116, v212, v213\n\t"
      "v_pk_mul_f32 v[222:223], v[214:215], v[208:209] op_sel_hi:[0,1]\n\t"
      "v_pk_fma_f32 v[214:215], v[214:215], v[208:209], v[222:223] op_sel:[1,1,0] op_sel_hi:[1,0,1] neg_lo:[1,0,0]\n\t"
      "v_cvt_pk_bf16_f32 v117, v214, v215\n\t"
      "v_pk_mul_f32 v[220:221], v[216:217], v[208:209] op_sel_hi:[0,1]\n\t"
      "v_pk_fma_f32 v[216:217], v[216:217], v[208:209], v[220:221] op_sel:[1,1,0] op_sel_hi:[1,0,1] neg_lo:[1,0,0]\n\t"
      "v_cvt_pk_bf16_f32 v118, v216, v217\n\t"
      "v_pk_mul_f32 v[222:223], v[218:219], v[208:209] op_sel_hi:[0,1]\n\t"
      "v_pk_fma_f32 v[218:219], v[218:219], v[208:209], v[222:223] op_sel:[1,1,0] op_sel_hi:[1,0,1] neg_lo:[1,0,0]\n\t"
      "v_cvt_pk_bf16_f32 v119, v218, v219\n\t"
      "v_pk_mul_f32 v[220:221], v[212:213], v[208:209] op_sel_hi:[0,1]\n\t"
      "v_pk_fma_f32 v[212:213], v[212:213], v[208:209], v[220:221] op_sel:[1,1,0] op_sel_hi:[1,0,1] neg_lo:[1,0,0]\n\t"
      "v_cvt_pk_bf16_f32 v120, v212, v213\n\t"
      "v_pk_mul_f32 v[222:223], v[214:215], v[208:209] op_sel_hi:[0,1]\n\t"
      "v_pk_fma_f32 v[214:215], v[214:215], v[208:209], v[222:223] op_sel:[1,1,0] op_sel_hi:[1,0,1] neg_lo:[1,0,0]\n\t"
      "v_cvt_pk_bf16_f32 v121, v214, v215\n\t"
      "v_pk_mul_f32 v[220:221], v[216:217], v[208:209] op_sel_hi:[0,1]\n\t"
      "v_pk_fma_f32 v[216:217], v[216:217], v[208:209], v[220:221] op_sel:[1,1,0] op_sel_hi:[1,0,1] neg_lo:[1,0,0]\n\t"
      "v_cvt_pk_bf16_f32 v122, v216, v217\n\t"
      "v_pk_mul_f32 v[222:223], v[218:219], v[208:209] op_sel_hi:[0,1]\n\t"
      "v_pk_fma_f32 v[218:219], v[218:219], v[208:209], v[222:223] op_sel:[1,1,0] op_sel_hi:[1,0,1] neg_lo:[1,0,0]\n\t"
      "v_cvt_pk_bf16_f32 v123, v218, v219\n\t"
      "v_pk_mul_f32 v[220:221], v[212:213], v[208:209] op_sel_hi:[0,1]\n\t"
      "v_pk_fma_f32 v[212:213], v[212:213], v[208:209], v[220:221] op_sel:[1,1,0] op_sel_hi:[1,0,1] neg_lo:[1,0,0]\n\t"
      "v_cvt_pk_bf16_f32 v124, v212, v213\n\t"
      "v_pk_mul_f32 v[222:223], v[214:215], v[208:209] op_sel_hi:[0,1]\n\t"
      "v_pk_fma_f32 v[214:215], v[214:215], v[208:209], v[222:223] op_sel:[1,1,0] op_sel_hi:[1,0,1] neg_lo:[1,0,0]\n\t"
      "v_cvt_pk_bf16_f32 v125, v214, v215\n\t"
      "v_pk_mul_f32 v[220:221], v[216:217], v[208:209] op_sel_hi:[0,1]\n\t"
      "v_pk_fma_f32 v[216:217], v[216:217], v[208:209], v[220:221] op_sel:[1,1,0] op_sel_hi:[1,0,1] neg_lo:[1,0,0]\n\t"
      "v_cvt_pk_bf16_f32 v126, v216, v217\n\t"
      "v_pk_mul_f32 v[222:223], v[218:219], v[208:209] op_sel_hi:[0,1]\n\t"
      "v_pk_fma_f32 v[218:219], v[218:219], v[208:209], v[222:223] op_sel:[1,1,0] op_sel_hi:[1,0,1] neg_lo:[1,0,0]\n\t"
      "v_cvt_pk_bf16_f32 v127, v218, v219\n\t"
      "v_pk_mul_f32 v[220:221], v[212:213], v[208:209] op_sel_hi:[0,1]\n\t"
      "v_pk_fma_f32 v[212:213], v[212:213], v[208:209], v[220:221] op_sel:[1,1,0] op_sel_hi:[1,0,1] neg_lo:[1,0,0]\n\t"
      "v_cvt_pk_bf16_f32 v128, v212, v213\n\t"
      "v_pk_mul_f32 v[222:223], v[214:215], v[208:209] op_sel_hi:[0,1]\n\t"
      "v_pk_fma_f32 v[214:215], v[214:215], v[208:209], v[222:223] op_sel:[1,1,0] op_sel_hi:[1,0,1] neg_lo:[1,0,0]\n\t"
      "v_cvt_pk_bf16_f32 v129, v214, v215\n\t"
      "v_pk_mul_f32 v[220:221], v[216:217], v[208:209] op_sel_hi:[0,1]\n\t"
      "v_pk_fma_f32 v[216:217], v[216:217], v[208:209], v[220:221] op_sel:[1,1,0] op_sel_hi:[1,0,1] neg_lo:[1,0,0]\n\t"
      "v_cvt_pk_bf16_f32 v130, v216, v217\n\t"
      "v_pk_mul_f32 v[222:223], v[218:219], v[208:209] op_sel_hi:[0,1]\n\t"
      "v_pk_fma_f32 v[218:219], v[218:219], v[208:209], v[222:223] op_sel:[1,1,0] op_sel_hi:[1,0,1] neg_lo:[1,0,0]\n\t"
      "v_cvt_pk_bf16_f32 v131, v218, v219\n\t"
      "v_cvt_pk_bf16_f32 v132, v202, -v203\n\t"
      "v_pk_mul_f32 v[222:223], v[202:203], v[204:205] op_sel_hi:[0,1]\n\t"
      "v_pk_fma_f32 v[234:235], v[202:203], v[204:205], v[222:223] op_sel:[1,1,0] op_sel_hi:[1,0,1] neg_lo:[1,0,0]\n\t"
      "v_cvt_pk_bf16_f32 v133, v234, -v235\n\t"
      "v_pk_mul_f32 v[220:221], v[234:235], v[204:205] op_sel_hi:[0,1]\n\t"
      "v_pk_fma_f32 v[228:229], v[234:235], v[204:205], v[220:221] op_sel:[1,1,0] op_sel_hi:[1,0,1] neg_lo:[1,0,0]\n\t"
      "v_cvt_pk_bf16_f32 v134, v228, -v229\n\t"
      "v_pk_mul_f32 v[222:223], v[228:229], v[204:205] op_sel_hi:[0,1]\n\t"
      "v_pk_fma_f32 v[236:237], v[228:229], v[204:205], v[222:223] op_sel:[1,1,0] op_sel_hi:[1,0,1] neg_lo:[1,0,0]\n\t"
      "v_cvt_pk_bf16_f32 v135, v236, -v237\n\t"
      "v_pk_mul_f32 v[220:221], v[236:237], v[204:205] op_sel_hi:[0,1]\n\t"
      "v_pk_fma_f32 v[230:231], v[236:237], v[204:205], v[220:221] op_sel:[1,1,0] op_sel_hi:[1,0,1] neg_lo:[1,0,0]\n\t"
      "v_cvt_pk_bf16_f32 v136, v230, -v231\n\t"
      "v_pk_mul_f32 v[222:223], v[230:231], v[204:205] op_sel_hi:[0,1]\n\t"
      "v_pk_fma_f32 v[234:235], v[230:231], v[204:205], v[222:223] op_sel:[1,1,0] op_sel_hi:[1,0,1] neg_lo:[1,0,0]\n\t"
      "v_cvt_pk_bf16_f32 v137, v234, -v235\n\t"
      "v_pk_mul_f32 v[220:221], v[234:235], v[204:205] op_sel_hi:[0,1]\n\t"
      "v_pk_fma_f32 v[232:233], v[234:235], v[204:205], v[220:221] op_sel:[1,1,0] op_sel_hi:[1,0,1] neg_lo:[1,0,0]\n\t"
      "v_cvt_pk_bf16_f32 v138, v232, -v233\n\t"
      "v_pk_mul_f32 v[222:223], v[232:233], v[204:205] op_sel_hi:[0,1]\n\t"
      "v_pk_fma_f32 v[236:237], v[232:233], v[204:205], v[222:223] op_sel:[1,1,0] op_sel_hi:[1,0,1] neg_lo:[1,0,0]\n\t"
      "v_cvt_pk_bf16_f32 v139, v236, -v237\n\t"
      "v_pk_mul_f32 v[220:221], v[202:203], v[210:211] op_sel_hi:[0,1]\n\t"
      "v_pk_fma_f32 v[212:213], v[202:203], v[210:211], v[220:221] op_sel:[1,1,0] op_sel_hi:[1,0,1] neg_lo:[1,0,0]\n\t"
      "v_cvt_pk_bf16_f32 v140, v212, -v213\n\t"
      "v_pk_mul_f32 v[222:223], v[228:229], v[210:211] op_sel_hi:[0,1]\n\t"
      "v_pk_fma_f32 v[234:235], v[228:229], v[210:211], v[222:223] op_sel:[1,1,0] op_sel_hi:[1,0,1] neg_lo:[1,0,0]\n\t"
      "v_cvt_pk_bf16_f32 v141, v234, -v235\n\t"
      "v_pk_mul_f32 v[220:221], v[230:231], v[210:211] op_sel_hi:[0,1]\n\t"
      "v_pk_fma_f32 v[214:215], v[230:231], v[210:211], v[220:221] op_sel:[1,1,0] op_sel_hi:[1,0,1] neg_lo:[1,0,0]\n\t"
      "v_cvt_pk_bf16_f32 v142, v214, -v215\n\t"
      "v_pk_mul_f32 v[222:223], v[232:233], v[210:211] op_sel_hi:[0,1]\n\t"
      "v_pk_fma_f32 v[236:237], v[232:233], v[210:211], v[222:223] op_sel:[1,1,0] op_sel_hi:[1,0,1] neg_lo:[1,0,0]\n\t"
      "v_cvt_pk_bf16_f32 v143, v236, -v237\n\t"
      "v_pk_mul_f32 v[220:221], v[212:213], v[210:211] op_sel_hi:[0,1]\n\t"
      "v_pk_fma_f32 v[212:213], v[212:213], v[210:211], v[220:221] op_sel:[1,1,0] op_sel_hi:[1,0,1] neg_lo:[1,0,0]\n\t"
      "v_cvt_pk_bf16_f32 v144, v212, -v213\n\t"
      "v_pk_mul_f32 v[222:223], v[214:215], v[210:211] op_sel_hi:[0,1]\n\t"
      "v_pk_fma_f32 v[214:215], v[214:215], v[210:211], v[222:223] op_sel:[1,1,0] op_sel_hi:[1,0,1] neg_lo:[1,0,0]\n\t"
      "v_cvt_pk_bf16_f32 v145, v214, -v215\n\t"
      "v_pk_mul_f32 v[220:221], v[212:213], v[210:211] op_sel_hi:[0,1]\n\t"
      "v_pk_fma_f32 v[212:213], v[212:213], v[210:211], v[220:221] op_sel:[1,1,0] op_sel_hi:[1,0,1] neg_lo:[1,0,0]\n\t"
      "v_cvt_pk_bf16_f32 v146, v212, -v213\n\t"
      "v_pk_mul_f32 v[222:223], v[214:215], v[210:211] op_sel_hi:[0,1]\n\t"
      "v_pk_fma_f32 v[214:215], v[214:215], v[210:211], v[222:223] op_sel:[1,1,0] op_sel_hi:[1,0,1] neg_lo:[1,0,0]\n\t"
      "v_cvt_pk_bf16_f32 v147, v214, -v215\n\t"
      "v_pk_mul_f32 v[220:221], v[212:213], v[210:211] op_sel_hi:[0,1]\n\t"
      "v_pk_fma_f32 v[212:213], v[212:213], v[210:211], v[220:221] op_sel:[1,1,0] op_sel_hi:[1,0,1] neg_lo:[1,0,0]\n\t"
      "v_cvt_pk_bf16_f32 v148, v212, -v213\n\t"
      "v_pk_mul_f32 v[222:223], v[212:213], v[210:211] op_sel_hi:[0,1]\n\t"
      "v_pk_fma_f32 v[212:213], v[212:213], v[210:211], v[222:223] op_sel:[1,1,0] op_sel_hi:[1,0,1] neg_lo:[1,0,0]\n\t"
      "v_cvt_pk_bf16_f32 v149, v212, -v213\n\t"
      "v_pk_mul_f32 v[220:221], v[212:213], v[210:211] op_sel_hi:[0,1]\n\t"
      "v_pk_fma_f32 v[212:213], v[212:213], v[210:211], v[220:221] op_sel:[1,1,0] op_sel_hi:[1,0,1] neg_lo:[1,0,0]\n\t"
      "v_cvt_pk_bf16_f32 v150, v212, -v213\n\t"
      "v_pk_mul_f32 v[222:223], v[212:213], v[210:211] op_sel_hi:[0,1]\n\t"
      "v_pk_fma_f32 v[212:213], v[212:213], v[210:211], v[222:223] op_sel:[1,1,0] op_sel_hi:[1,0,1] neg_lo:[1,0,0]\n\t"
      "v_cvt_pk_bf16_f32 v151, v212, -v213\n\t"
      "v_pk_mul_f32 v[220:221], v[212:213], v[210:211] op_sel_hi:[0,1]\n\t"
      "v_pk_fma_f32 v[212:213], v[212:213], v[210:211], v[220:221] op_sel:[1,1,0] op_sel_hi:[1,0,1] neg_lo:[1,0,0]\n\t"
      "v_cvt_pk_bf16_f32 v152, v212, -v213\n\t"
      "s_setprio 0\n\t"
      "s_barrier\n\t"
      "s_mov_b64 s[66:67], exec\n\t"
      "s_mov_b64 exec, s[74:75]\n\t"
      "s_nop 0\n\t"
      "buffer_load_dword v27, v46, s[52:55], 0 offen\n\t"
      "s_mov_b64 exec, s[66:67]\n\t"
      "s_mov_b32 vcc_lo, 0x55555555\n\t"
      "s_mov_b32 vcc_hi, 0x55555555\n\t"
      "v_cndmask_b32_dpp v64, v101, v100, vcc quad_perm:[1,0,3,2] row_mask:0xf bank_mask:0xf bound_ctrl:1\n\t"
      "v_cndmask_b32_dpp v66, v103, v102, vcc quad_perm:[1,0,3,2] row_mask:0xf bank_mask:0xf bound_ctrl:1\n\t"
      "v_cndmask_b32_dpp v68, v105, v104, vcc quad_perm:[1,0,3,2] row_mask:0xf bank_mask:0xf bound_ctrl:1\n\t"
      "v_cndmask_b32_dpp v70, v107, v106, vcc quad_perm:[1,0,3,2] row_mask:0xf bank_mask:0xf bound_ctrl:1\n\t"
      "v_cndmask_b32_dpp v72, v109, v108, vcc quad_perm:[1,0,3,2] row_mask:0xf bank_mask:0xf bound_ctrl:1\n\t"
      "v_cndmask_b32_dpp v74, v111, v110, vcc quad_perm:[1,0,3,2] row_mask:0xf bank_mask:0xf bound_ctrl:1\n\t"
      "v_cndmask_b32_dpp v76, v113, v112, vcc quad_perm:[1,0,3,2] row_mask:0xf bank_mask:0xf bound_ctrl:1\n\t"
      "v_cndmask_b32_dpp v78, v115, v114, vcc quad_perm:[1,0,3,2] row_mask:0xf bank_mask:0xf bound_ctrl:1\n\t"
      "v_cndmask_b32_dpp v80, v117, v116, vcc quad_perm:[1,0,3,2] row_mask:0xf bank_mask:0xf bound_ctrl:1\n\t"
      "v_cndmask_b32_dpp v82, v119, v118, vcc quad_perm:[1,0,3,2] row_mask:0xf bank_mask:0xf bound_ctrl:1\n\t"
      "v_cndmask_b32_dpp v84, v121, v120, vcc quad_perm:[1,0,3,2] row_mask:0xf bank_mask:0xf bound_ctrl:1\n\t"
      "v_cndmask_b32_dpp v86, v123, v122, vcc quad_perm:[1,0,3,2] row_mask:0xf bank_mask:0xf bound_ctrl:1\n\t"
      "v_cndmask_b32_dpp v88, v125, v124, vcc quad_perm:[1,0,3,2] row_mask:0xf bank_mask:0xf bound_ctrl:1\n\t"
      "v_cndmask_b32_dpp v90, v127, v126, vcc quad_perm:[1,0,3,2] row_mask:0xf bank_mask:0xf bound_ctrl:1\n\t"
      "v_cndmask_b32_dpp v92, v129, v128, vcc quad_perm:[1,0,3,2] row_mask:0xf bank_mask:0xf bound_ctrl:1\n\t"
      "v_cndmask_b32_dpp v94, v131, v130, vcc quad_perm:[1,0,3,2] row_mask:0xf bank_mask:0xf bound_ctrl:1\n\t"
      "s_not_b64 vcc, vcc\n\t"
      "v_cndmask_b32_dpp v65, v100, v101, vcc quad_perm:[1,0,3,2] row_mask:0xf bank_mask:0xf bound_ctrl:1\n\t"
      "v_cndmask_b32_dpp v67, v102, v103, vcc quad_perm:[1,0,3,2] row_mask:0xf bank_mask:0xf bound_ctrl:1\n\t"
      "v_cndmask_b32_dpp v69, v104, v105, vcc quad_perm:[1,0,3,2] row_mask:0xf bank_mask:0xf bound_ctrl:1\n\t"
      "v_cndmask_b32_dpp v71, v106, v107, vcc quad_perm:[1,0,3,2] row_mask:0xf bank_mask:0xf bound_ctrl:1\n\t"
      "v_cndmask_b32_dpp v73, v108, v109, vcc quad_perm:[1,0,3,2] row_mask:0xf bank_mask:0xf bound_ctrl:1\n\t"
      "v_cndmask_b32_dpp v75, v110, v111, vcc quad_perm:[1,0,3,2] row_mask:0xf bank_mask:0xf bound_ctrl:1\n\t"
      "v_cndmask_b32_dpp v77, v112, v113, vcc quad_perm:[1,0,3,2] row_mask:0xf bank_mask:0xf bound_ctrl:1\n\t"
      "v_cndmask_b32_dpp v79, v114, v115, vcc quad_perm:[1,0,3,2] row_mask:0xf bank_mask:0xf bound_ctrl:1\n\t"
      "v_cndmask_b32_dpp v81, v116, v117, vcc quad_perm:[1,0,3,2] row_mask:0xf bank_mask:0xf bound_ctrl:1\n\t"
      "v_cndmask_b32_dpp v83, v118, v119, vcc quad_perm:[1,0,3,2] row_mask:0xf bank_mask:0xf bound_ctrl:1\n\t"
      "v_cndmask_b32_dpp v85, v120, v121, vcc quad_perm:[1,0,3,2] row_mask:0xf bank_mask:0xf bound_ctrl:1\n\t"
      "v_cndmask_b32_dpp v87, v122, v123, vcc quad_perm:[1,0,3,2] row_mask:0xf bank_mask:0xf bound_ctrl:1\n\t"
      "v_cndmask_b32_dpp v89, v124, v125, vcc quad_perm:[1,0,3,2] row_mask:0xf bank_mask:0xf bound_ctrl:1\n\t"
      "v_cndmask_b32_dpp v91, v126, v127, vcc quad_perm:[1,0,3,2] row_mask:0xf bank_mask:0xf bound_ctrl:1\n\t"
      "v_cndmask_b32_dpp v93, v128, v129, vcc quad_perm:[1,0,3,2] row_mask:0xf bank_mask:0xf bound_ctrl:1\n\t"
      "v_cndmask_b32_dpp v95, v130, v131, vcc quad_perm:[1,0,3,2] row_mask:0xf bank_mask:0xf bound_ctrl:1\n\t"
      "s_mov_b32 vcc_lo, 0x33333333\n\t"
      "s_mov_b32 vcc_hi, 0x33333333\n\t"
      "v_cndmask_b32_dpp v100, v66, v64, vcc quad_perm:[2,3,0,1] row_mask:0xf bank_mask:0xf bound_ctrl:1\n\t"
      "v_cndmask_b32_dpp v101, v67, v65, vcc quad_perm:[2,3,0,1] row_mask:0xf bank_mask:0xf bound_ctrl:1\n\t"
      "v_cndmask_b32_dpp v104, v70, v68, vcc quad_perm:[2,3,0,1] row_mask:0xf bank_mask:0xf bound_ctrl:1\n\t"
      "v_cndmask_b32_dpp v105, v71, v69, vcc quad_perm:[2,3,0,1] row_mask:0xf bank_mask:0xf bound_ctrl:1\n\t"
      "v_cndmask_b32_dpp v108, v74, v72, vcc quad_perm:[2,3,0,1] row_mask:0xf bank_mask:0xf bound_ctrl:1\n\t"
      "v_cndmask_b32_dpp v109, v75, v73, vcc quad_perm:[2,3,0,1] row_mask:0xf bank_mask:0xf bound_ctrl:1\n\t"
      "v_cndmask_b32_dpp v112, v78, v76, vcc quad_perm:[2,3,0,1] row_mask:0xf bank_mask:0xf bound_ctrl:1\n\t"
      "v_cndmask_b32_dpp v113, v79, v77, vcc quad_perm:[2,3,0,1] row_mask:0xf bank_mask:0xf bound_ctrl:1\n\t"
      "v_cndmask_b32_dpp v116, v82, v80, vcc quad_perm:[2,3,0,1] row_mask:0xf bank_mask:0xf bound_ctrl:1\n\t"
      "v_cndmask_b32_dpp v117, v83, v81, vcc quad_perm:[2,3,0,1] row_mask:0xf bank_mask:0xf bound_ctrl:1\n\t"
      "v_cndmask_b32_dpp v120, v86, v84, vcc quad_perm:[2,3,0,1] row_mask:0xf bank_mask:0xf bound_ctrl:1\n\t"
      "v_cndmask_b32_dpp v121, v87, v85, vcc quad_perm:[2,3,0,1] row_mask:0xf bank_mask:0xf bound_ctrl:1\n\t"
      "v_cndmask_b32_dpp v124, v90, v88, vcc quad_perm:[2,3,0,1] row_mask:0xf bank_mask:0xf bound_ctrl:1\n\t"
      "v_cndmask_b32_dpp v125, v91, v89, vcc quad_perm:[2,3,0,1] row_mask:0xf bank_mask:0xf bound_ctrl:1\n\t"
      "v_cndmask_b32_dpp v128, v94, v92, vcc quad_perm:[2,3,0,1] row_mask:0xf bank_mask:0xf bound_ctrl:1\n\t"
      "v_cndmask_b32_dpp v129, v95, v93, vcc quad_perm:[2,3,0,1] row_mask:0xf bank_mask:0xf bound_ctrl:1\n\t"
      "s_not_b64 vcc, vcc\n\t"
      "v_cndmask_b32_dpp v102, v64, v66, vcc quad_perm:[2,3,0,1] row_mask:0xf bank_mask:0xf bound_ctrl:1\n\t"
      "v_cndmask_b32_dpp v103, v65, v67, vcc quad_perm:[2,3,0,1] row_mask:0xf bank_mask:0xf bound_ctrl:1\n\t"
      "v_cndmask_b32_dpp v106, v68, v70, vcc quad_perm:[2,3,0,1] row_mask:0xf bank_mask:0xf bound_ctrl:1\n\t"
      "v_cndmask_b32_dpp v107, v69, v71, vcc quad_perm:[2,3,0,1] row_mask:0xf bank_mask:0xf bound_ctrl:1\n\t"
      "v_cndmask_b32_dpp v110, v72, v74, vcc quad_perm:[2,3,0,1] row_mask:0xf bank_mask:0xf bound_ctrl:1\n\t"
      "v_cndmask_b32_dpp v111, v73, v75, vcc quad_perm:[2,3,0,1] row_mask:0xf bank_mask:0xf bound_ctrl:1\n\t"
      "v_cndmask_b32_dpp v114, v76, v78, vcc quad_perm:[2,3,0,1] row_mask:0xf bank_mask:0xf bound_ctrl:1\n\t"
      "v_cndmask_b32_dpp v115, v77, v79, vcc quad_perm:[2,3,0,1] row_mask:0xf bank_mask:0xf bound_ctrl:1\n\t"
      "v_cndmask_b32_dpp v118, v80, v82, vcc quad_perm:[2,3,0,1] row_mask:0xf bank_mask:0xf bound_ctrl:1\n\t"
      "v_cndmask_b32_dpp v119, v81, v83, vcc quad_perm:[2,3,0,1] row_mask:0xf bank_mask:0xf bound_ctrl:1\n\t"
      "v_cndmask_b32_dpp v122, v84, v86, vcc quad_perm:[2,3,0,1] row_mask:0xf bank_mask:0xf bound_ctrl:1\n\t"
      "v_cndmask_b32_dpp v123, v85, v87, vcc quad_perm:[2,3,0,1] row_mask:0xf bank_mask:0xf bound_ctrl:1\n\t"
      "v_cndmask_b32_dpp v126, v88, v90, vcc quad_perm:[2,3,0,1] row_mask:0xf bank_mask:0xf bound_ctrl:1\n\t"
      "v_cndmask_b32_dpp v127, v89, v91, vcc quad_perm:[2,3,0,1] row_mask:0xf bank_mask:0xf bound_ctrl:1\n\t"
      "v_cndmask_b32_dpp v130, v92, v94, vcc quad_perm:[2,3,0,1] row_mask:0xf bank_mask:0xf bound_ctrl:1\n\t"
      "v_cndmask_b32_dpp v131, v93, v95, vcc quad_perm:[2,3,0,1] row_mask:0xf bank_mask:0xf bound_ctrl:1\n\t"
      "s_barrier\n\t"
      "s_mov_b32 vcc_lo, 0xf0f0f0f\n\t"
      "s_mov_b32 vcc_hi, 0xf0f0f0f\n\t"
      "v_cndmask_b32_dpp v64, v104, v100, vcc row_shr:4 row_mask:0xf bank_mask:0xf bound_ctrl:1\n\t"
      "v_cndmask_b32_dpp v65, v105, v101, vcc row_shr:4 row_mask:0xf bank_mask:0xf bound_ctrl:1\n\t"
      "v_cndmask_b32_dpp v66, v106, v102, vcc row_shr:4 row_mask:0xf bank_mask:0xf bound_ctrl:1\n\t"
      "v_cndmask_b32_dpp v67, v107, v103, vcc row_shr:4 row_mask:0xf bank_mask:0xf bound_ctrl:1\n\t"
      "v_cndmask_b32_dpp v72, v112, v108, vcc row_shr:4 row_mask:0xf bank_mask:0xf bound_ctrl:1\n\t"
      "v_cndmask_b32_dpp v73, v113, v109, vcc row_shr:4 row_mask:0xf bank_mask:0xf bound_ctrl:1\n\t"
      "v_cndmask_b32_dpp v74, v114, v110, vcc row_shr:4 row_mask:0xf bank_mask:0xf bound_ctrl:1\n\t"
      "v_cndmask_b32_dpp v75, v115, v111, vcc row_shr:4 row_mask:0xf bank_mask:0xf bound_ctrl:1\n\t"
      "v_cndmask_b32_dpp v80, v120, v116, vcc row_shr:4 row_mask:0xf bank_mask:0xf bound_ctrl:1\n\t"
      "v_cndmask_b32_dpp v81, v121, v117, vcc row_shr:4 row_mask:0xf bank_mask:0xf bound_ctrl:1\n\t"
      "v_cndmask_b32_dpp v82, v122, v118, vcc row_shr:4 row_mask:0xf bank_mask:0xf bound_ctrl:1\n\t"
      "v_cndmask_b32_dpp v83, v123, v119, vcc row_shr:4 row_mask:0xf bank_mask:0xf bound_ctrl:1\n\t"
      "v_cndmask_b32_dpp v88, v128, v124, vcc row_shr:4 row_mask:0xf bank_mask:0xf bound_ctrl:1\n\t"
      "v_cndmask_b32_dpp v89, v129, v125, vcc row_shr:4 row_mask:0xf bank_mask:0xf bound_ctrl:1\n\t"
      "v_cndmask_b32_dpp v90, v130, v126, vcc row_shr:4 row_mask:0xf bank_mask:0xf bound_ctrl:1\n\t"
      "v_cndmask_b32_dpp v91, v131, v127, vcc row_shr:4 row_mask:0xf bank_mask:0xf bound_ctrl:1\n\t"
      "s_not_b64 vcc, vcc\n\t"
      "v_cndmask_b32_dpp v68, v100, v104, vcc row_shl:4 row_mask:0xf bank_mask:0xf bound_ctrl:1\n\t"
      "v_cndmask_b32_dpp v69, v101, v105, vcc row_shl:4 row_mask:0xf bank_mask:0xf bound_ctrl:1\n\t"
      "v_cndmask_b32_dpp v70, v102, v106, vcc row_shl:4 row_mask:0xf bank_mask:0xf bound_ctrl:1\n\t"
      "v_cndmask_b32_dpp v71, v103, v107, vcc row_shl:4 row_mask:0xf bank_mask:0xf bound_ctrl:1\n\t"
      "v_cndmask_b32_dpp v76, v108, v112, vcc row_shl:4 row_mask:0xf bank_mask:0xf bound_ctrl:1\n\t"
      "v_cndmask_b32_dpp v77, v109, v113, vcc row_shl:4 row_mask:0xf bank_mask:0xf bound_ctrl:1\n\t"
      "v_cndmask_b32_dpp v78, v110, v114, vcc row_shl:4 row_mask:0xf bank_mask:0xf bound_ctrl:1\n\t"
      "v_cndmask_b32_dpp v79, v111, v115, vcc row_shl:4 row_mask:0xf bank_mask:0xf bound_ctrl:1\n\t"
      "v_cndmask_b32_dpp v84, v116, v120, vcc row_shl:4 row_mask:0xf bank_mask:0xf bound_ctrl:1\n\t"
      "v_cndmask_b32_dpp v85, v117, v121, vcc row_shl:4 row_mask:0xf bank_mask:0xf bound_ctrl:1\n\t"
      "v_cndmask_b32_dpp v86, v118, v122, vcc row_shl:4 row_mask:0xf bank_mask:0xf bound_ctrl:1\n\t"
      "v_cndmask_b32_dpp v87, v119, v123, vcc row_shl:4 row_mask:0xf bank_mask:0xf bound_ctrl:1\n\t"
      "v_cndmask_b32_dpp v92, v124, v128, vcc row_shl:4 row_mask:0xf bank_mask:0xf bound_ctrl:1\n\t"
      "v_cndmask_b32_dpp v93, v125, v129, vcc row_shl:4 row_mask:0xf bank_mask:0xf bound_ctrl:1\n\t"
      "v_cndmask_b32_dpp v94, v126, v130, vcc row_shl:4 row_mask:0xf bank_mask:0xf bound_ctrl:1\n\t"
      "v_cndmask_b32_dpp v95, v127, v131, vcc row_shl:4 row_mask:0xf bank_mask:0xf bound_ctrl:1\n\t"
      "s_mov_b32 vcc_lo, 0xff00ff\n\t"
      "s_mov_b32 vcc_hi, 0xff00ff\n\t"
      "v_cndmask_b32_dpp v100, v72, v64, vcc row_ror:8 row_mask:0xf bank_mask:0xf bound_ctrl:1\n\t"
      "v_cndmask_b32_dpp v101, v73, v65, vcc row_ror:8 row_mask:0xf bank_mask:0xf bound_ctrl:1\n\t"
      "v_cndmask_b32_dpp v102, v74, v66, vcc row_ror:8 row_mask:0xf bank_mask:0xf bound_ctrl:1\n\t"
      "v_cndmask_b32_dpp v103, v75, v67, vcc row_ror:8 row_mask:0xf bank_mask:0xf bound_ctrl:1\n\t"
      "v_cndmask_b32_dpp v104, v76, v68, vcc row_ror:8 row_mask:0xf bank_mask:0xf bound_ctrl:1\n\t"
      "v_cndmask_b32_dpp v105, v77, v69, vcc row_ror:8 row_mask:0xf bank_mask:0xf bound_ctrl:1\n\t"
      "v_cndmask_b32_dpp v106, v78, v70, vcc row_ror:8 row_mask:0xf bank_mask:0xf bound_ctrl:1\n\t"
      "v_cndmask_b32_dpp v107, v79, v71, vcc row_ror:8 row_mask:0xf bank_mask:0xf bound_ctrl:1\n\t"
      "v_cndmask_b32_dpp v116, v88, v80, vcc row_ror:8 row_mask:0xf bank_mask:0xf bound_ctrl:1\n\t"
      "v_cndmask_b32_dpp v117, v89, v81, vcc row_ror:8 row_mask:0xf bank_mask:0xf bound_ctrl:1\n\t"
      "v_cndmask_b32_dpp v118, v90, v82, vcc row_ror:8 row_mask:0xf bank_mask:0xf bound_ctrl:1\n\t"
      "v_cndmask_b32_dpp v119, v91, v83, vcc row_ror:8 row_mask:0xf bank_mask:0xf bound_ctrl:1\n\t"
      "v_cndmask_b32_dpp v120, v92, v84, vcc row_ror:8 row_mask:0xf bank_mask:0xf bound_ctrl:1\n\t"
      "v_cndmask_b32_dpp v121, v93, v85, vcc row_ror:8 row_mask:0xf bank_mask:0xf bound_ctrl:1\n\t"
      "v_cndmask_b32_dpp v122, v94, v86, vcc row_ror:8 row_mask:0xf bank_mask:0xf bound_ctrl:1\n\t"
      "v_cndmask_b32_dpp v123, v95, v87, vcc row_ror:8 row_mask:0xf bank_mask:0xf bound_ctrl:1\n\t"
      "s_not_b64 vcc, vcc\n\t"
      "v_cndmask_b32_dpp v108, v64, v72, vcc row_ror:8 row_mask:0xf bank_mask:0xf bound_ctrl:1\n\t"
      "v_cndmask_b32_dpp v109, v65, v73, vcc row_ror:8 row_mask:0xf bank_mask:0xf bound_ctrl:1\n\t"
      "v_cndmask_b32_dpp v110, v66, v74, vcc row_ror:8 row_mask:0xf bank_mask:0xf bound_ctrl:1\n\t"
      "v_cndmask_b32_dpp v111, v67, v75, vcc row_ror:8 row_mask:0xf bank_mask:0xf bound_ctrl:1\n\t"
      "v_cndmask_b32_dpp v112, v68, v76, vcc row_ror:8 row_mask:0xf bank_mask:0xf bound_ctrl:1\n\t"
      "v_cndmask_b32_dpp v113, v69, v77, vcc row_ror:8 row_mask:0xf bank_mask:0xf bound_ctrl:1\n\t"
      "v_cndmask_b32_dpp v114, v70, v78, vcc row_ror:8 row_mask:0xf bank_mask:0xf bound_ctrl:1\n\t"
      "v_cndmask_b32_dpp v115, v71, v79, vcc row_ror:8 row_mask:0xf bank_mask:0xf bound_ctrl:1\n\t"
      "v_cndmask_b32_dpp v124, v80, v88, vcc row_ror:8 row_mask:0xf bank_mask:0xf bound_ctrl:1\n\t"
      "v_cndmask_b32_dpp v125, v81, v89, vcc row_ror:8 row_mask:0xf bank_mask:0xf bound_ctrl:1\n\t"
      "v_cndmask_b32_dpp v126, v82, v90, vcc row_ror:8 row_mask:0xf bank_mask:0xf bound_ctrl:1\n\t"
      "v_cndmask_b32_dpp v127, v83, v91, vcc row_ror:8 row_mask:0xf bank_mask:0xf bound_ctrl:1\n\t"
      "v_cndmask_b32_dpp v128, v84, v92, vcc row_ror:8 row_mask:0xf bank_mask:0xf bound_ctrl:1\n\t"
      "v_cndmask_b32_dpp v129, v85, v93, vcc row_ror:8 row_mask:0xf bank_mask:0xf bound_ctrl:1\n\t"
      "v_cndmask_b32_dpp v130, v86, v94, vcc row_ror:8 row_mask:0xf bank_mask:0xf bound_ctrl:1\n\t"
      "v_cndmask_b32_dpp v131, v87, v95, vcc row_ror:8 row_mask:0xf bank_mask:0xf bound_ctrl:1\n\t"
      "v_permlane16_swap_b32 v100, v116\n\t"
      "s_barrier\n\t"
      "v_permlane16_swap_b32 v101, v117\n\t"
      "v_permlane16_swap_b32 v102, v118\n\t"
      "v_permlane16_swap_b32 v103, v119\n\t"
      "v_permlane16_swap_b32 v104, v120\n\t"
      "v_permlane16_swap_b32 v105, v121\n\t"
      "v_permlane16_swap_b32 v106, v122\n\t"
      "v_permlane16_swap_b32 v107, v123\n\t"
      "v_permlane16_swap_b32 v108, v124\n\t"
      "v_permlane16_swap_b32 v109, v125\n\t"
      "v_permlane16_swap_b32 v110, v126\n\t"
      "v_permlane16_swap_b32 v111, v127\n\t"
      "v_permlane16_swap_b32 v112, v128\n\t"
      "v_permlane16_swap_b32 v113, v129\n\t"
      "v_permlane16_swap_b32 v114, v130\n\t"
      "v_permlane16_swap_b32 v115, v131\n\t"
      "s_mov_b32 vcc_lo, 0x55555555\n\t"
      "s_mov_b32 vcc_hi, 0x55555555\n\t"
      "v_cndmask_b32_dpp v64, v133, v132, vcc quad_perm:[1,0,3,2] row_mask:0xf bank_mask:0xf bound_ctrl:1\n\t"
      "v_cndmask_b32_dpp v66, v135, v134, vcc quad_perm:[1,0,3,2] row_mask:0xf bank_mask:0xf bound_ctrl:1\n\t"
      "v_cndmask_b32_dpp v68, v137, v136, vcc quad_perm:[1,0,3,2] row_mask:0xf bank_mask:0xf bound_ctrl:1\n\t"
      "v_cndmask_b32_dpp v70, v139, v138, vcc quad_perm:[1,0,3,2] row_mask:0xf bank_mask:0xf bound_ctrl:1\n\t"
      "v_cndmask_b32_dpp v72, v141, v140, vcc quad_perm:[1,0,3,2] row_mask:0xf bank_mask:0xf bound_ctrl:1\n\t"
      "v_cndmask_b32_dpp v74, v143, v142, vcc quad_perm:[1,0,3,2] row_mask:0xf bank_mask:0xf bound_ctrl:1\n\t"
      "v_cndmask_b32_dpp v76, v145, v144, vcc quad_perm:[1,0,3,2] row_mask:0xf bank_mask:0xf bound_ctrl:1\n\t"
      "v_cndmask_b32_dpp v78, v147, v146, vcc quad_perm:[1,0,3,2] row_mask:0xf bank_mask:0xf bound_ctrl:1\n\t"
      "v_cndmask_b32_dpp v80, v149, v148, vcc quad_perm:[1,0,3,2] row_mask:0xf bank_mask:0xf bound_ctrl:1\n\t"
      "v_cndmask_b32_dpp v82, v151, v150, vcc quad_perm:[1,0,3,2] row_mask:0xf bank_mask:0xf bound_ctrl:1\n\t"
      "s_not_b64 vcc, vcc\n\t"
      "v_cndmask_b32_dpp v65, v132, v133, vcc quad_perm:[1,0,3,2] row_mask:0xf bank_mask:0xf bound_ctrl:1\n\t"
      "v_cndmask_b32_dpp v67, v134, v135, vcc quad_perm:[1,0,3,2] row_mask:0xf bank_mask:0xf bound_ctrl:1\n\t"
      "v_cndmask_b32_dpp v69, v136, v137, vcc quad_perm:[1,0,3,2] row_mask:0xf bank_mask:0xf bound_ctrl:1\n\t"
      "v_cndmask_b32_dpp v71, v138, v139, vcc quad_perm:[1,0,3,2] row_mask:0xf bank_mask:0xf bound_ctrl:1\n\t"
      "v_cndmask_b32_dpp v73, v140, v141, vcc quad_perm:[1,0,3,2] row_mask:0xf bank_mask:0xf bound_ctrl:1\n\t"
      "v_cndmask_b32_dpp v75, v142, v143, vcc quad_perm:[1,0,3,2] row_mask:0xf bank_mask:0xf bound_ctrl:1\n\t"
      "v_cndmask_b32_dpp v77, v144, v145, vcc quad_perm:[1,0,3,2] row_mask:0xf bank_mask:0xf bound_ctrl:1\n\t"
      "v_cndmask_b32_dpp v79, v146, v147, vcc quad_perm:[1,0,3,2] row_mask:0xf bank_mask:0xf bound_ctrl:1\n\t"
      "v_cndmask_b32_dpp v81, v148, v149, vcc quad_perm:[1,0,3,2] row_mask:0xf bank_mask:0xf bound_ctrl:1\n\t"
      "v_cndmask_b32_dpp v83, v150, v151, vcc quad_perm:[1,0,3,2] row_mask:0xf bank_mask:0xf bound_ctrl:1\n\t"
      "v_mov_b32_dpp v85, v152 quad_perm:[1,0,3,2] row_mask:0xf bank_mask:0xf bound_ctrl:1\n\t"
      "s_mov_b32 vcc_lo, 0x33333333\n\t"
      "s_mov_b32 vcc_hi, 0x33333333\n\t"
      "v_cndmask_b32_dpp v132, v66, v64, vcc quad_perm:[2,3,0,1] row_mask:0xf bank_mask:0xf bound_ctrl:1\n\t"
      "v_cndmask_b32_dpp v133, v67, v65, vcc quad_perm:[2,3,0,1] row_mask:0xf bank_mask:0xf bound_ctrl:1\n\t"
      "v_cndmask_b32_dpp v136, v70, v68, vcc quad_perm:[2,3,0,1] row_mask:0xf bank_mask:0xf bound_ctrl:1\n\t"
      "v_cndmask_b32_dpp v137, v71, v69, vcc quad_perm:[2,3,0,1] row_mask:0xf bank_mask:0xf bound_ctrl:1\n\t"
      "v_cndmask_b32_dpp v140, v74, v72, vcc quad_perm:[2,3,0,1] row_mask:0xf bank_mask:0xf bound_ctrl:1\n\t"
      "v_cndmask_b32_dpp v141, v75, v73, vcc quad_perm:[2,3,0,1] row_mask:0xf bank_mask:0xf bound_ctrl:1\n\t"
      "v_cndmask_b32_dpp v144, v78, v76, vcc quad_perm:[2,3,0,1] row_mask:0xf bank_mask:0xf bound_ctrl:1\n\t"
      "v_cndmask_b32_dpp v145, v79, v77, vcc quad_perm:[2,3,0,1] row_mask:0xf bank_mask:0xf bound_ctrl:1\n\t"
      "v_cndmask_b32_dpp v148, v82, v80, vcc quad_perm:[2,3,0,1] row_mask:0xf bank_mask:0xf bound_ctrl:1\n\t"
      "v_cndmask_b32_dpp v149, v83, v81, vcc quad_perm:[2,3,0,1] row_mask:0xf bank_mask:0xf bound_ctrl:1\n\t"
      "s_not_b64 vcc, vcc\n\t"
      "v_cndmask_b32_dpp v134, v64, v66, vcc quad_perm:[2,3,0,1] row_mask:0xf bank_mask:0xf bound_ctrl:1\n\t"
      "v_cndmask_b32_dpp v135, v65, v67, vcc quad_perm:[2,3,0,1] row_mask:0xf bank_mask:0xf bound_ctrl:1\n\t"
      "v_cndmask_b32_dpp v138, v68, v70, vcc quad_perm:[2,3,0,1] row_mask:0xf bank_mask:0xf bound_ctrl:1\n\t"
      "v_cndmask_b32_dpp v139, v69, v71, vcc quad_perm:[2,3,0,1] row_mask:0xf bank_mask:0xf bound_ctrl:1\n\t"
      "v_cndmask_b32_dpp v142, v72, v74, vcc quad_perm:[2,3,0,1] row_mask:0xf bank_mask:0xf bound_ctrl:1\n\t"
      "v_cndmask_b32_dpp v143, v73, v75, vcc quad_perm:[2,3,0,1] row_mask:0xf bank_mask:0xf bound_ctrl:1\n\t"
      "v_cndmask_b32_dpp v146, v76, v78, vcc quad_perm:[2,3,0,1] row_mask:0xf bank_mask:0xf bound_ctrl:1\n\t"
      "v_cndmask_b32_dpp v147, v77, v79, vcc quad_perm:[2,3,0,1] row_mask:0xf bank_mask:0xf bound_ctrl:1\n\t"
      "v_cndmask_b32_dpp v150, v80, v82, vcc quad_perm:[2,3,0,1] row_mask:0xf bank_mask:0xf bound_ctrl:1\n\t"
      "v_cndmask_b32_dpp v151, v81, v83, vcc quad_perm:[2,3,0,1] row_mask:0xf bank_mask:0xf bound_ctrl:1\n\t"
      "v_mov_b32_dpp v154, v152 quad_perm:[2,3,0,1] row_mask:0xf bank_mask:0xf bound_ctrl:1\n\t"
      "v_mov_b32_dpp v155, v85 quad_perm:[2,3,0,1] row_mask:0xf bank_mask:0xf bound_ctrl:1\n\t"
      "s_mov_b32 vcc_lo, 0xf0f0f0f\n\t"
      "s_mov_b32 vcc_hi, 0xf0f0f0f\n\t"
      "v_cndmask_b32_dpp v64, v136, v132, vcc row_shr:4 row_mask:0xf bank_mask:0xf bound_ctrl:1\n\t"
      "v_cndmask_b32_dpp v65, v137, v133, vcc row_shr:4 row_mask:0xf bank_mask:0xf bound_ctrl:1\n\t"
      "v_cndmask_b32_dpp v66, v138, v134, vcc row_shr:4 row_mask:0xf bank_mask:0xf bound_ctrl:1\n\t"
      "v_cndmask_b32_dpp v67, v139, v135, vcc row_shr:4 row_mask:0xf bank_mask:0xf bound_ctrl:1\n\t"
      "s_barrier\n\t"
      "v_cndmask_b32_dpp v72, v144, v140, vcc row_shr:4 row_mask:0xf bank_mask:0xf bound_ctrl:1\n\t"
      "v_cndmask_b32_dpp v73, v145, v141, vcc row_shr:4 row_mask:0xf bank_mask:0xf bound_ctrl:1\n\t"
      "v_cndmask_b32_dpp v74, v146, v142, vcc row_shr:4 row_mask:0xf bank_mask:0xf bound_ctrl:1\n\t"
      "v_cndmask_b32_dpp v75, v147, v143, vcc row_shr:4 row_mask:0xf bank_mask:0xf bound_ctrl:1\n\t"
      "v_cndmask_b32_dpp v80, v152, v148, vcc row_shr:4 row_mask:0xf bank_mask:0xf bound_ctrl:1\n\t"
      "v_cndmask_b32_dpp v81, v85, v149, vcc row_shr:4 row_mask:0xf bank_mask:0xf bound_ctrl:1\n\t"
      "v_cndmask_b32_dpp v82, v154, v150, vcc row_shr:4 row_mask:0xf bank_mask:0xf bound_ctrl:1\n\t"
      "v_cndmask_b32_dpp v83, v155, v151, vcc row_shr:4 row_mask:0xf bank_mask:0xf bound_ctrl:1\n\t"
      "s_not_b64 vcc, vcc\n\t"
      "v_cndmask_b32_dpp v68, v132, v136, vcc row_shl:4 row_mask:0xf bank_mask:0xf bound_ctrl:1\n\t"
      "v_cndmask_b32_dpp v69, v133, v137, vcc row_shl:4 row_mask:0xf bank_mask:0xf bound_ctrl:1\n\t"
      "v_cndmask_b32_dpp v70, v134, v138, vcc row_shl:4 row_mask:0xf bank_mask:0xf bound_ctrl:1\n\t"
      "v_cndmask_b32_dpp v71, v135, v139, vcc row_shl:4 row_mask:0xf bank_mask:0xf bound_ctrl:1\n\t"
      "v_cndmask_b32_dpp v76, v140, v144, vcc row_shl:4 row_mask:0xf bank_mask:0xf bound_ctrl:1\n\t"
      "v_cndmask_b32_dpp v77, v141, v145, vcc row_shl:4 row_mask:0xf bank_mask:0xf bound_ctrl:1\n\t"
      "v_cndmask_b32_dpp v78, v142, v146, vcc row_shl:4 row_mask:0xf bank_mask:0xf bound_ctrl:1\n\t"
      "v_cndmask_b32_dpp v79, v143, v147, vcc row_shl:4 row_mask:0xf bank_mask:0xf bound_ctrl:1\n\t"
      "v_cndmask_b32_dpp v84, v148, v152, vcc row_shl:4 row_mask:0xf bank_mask:0xf bound_ctrl:1\n\t"
      "v_cndmask_b32_dpp v153, v149, v85, vcc row_shl:4 row_mask:0xf bank_mask:0xf bound_ctrl:1\n\t"
      "v_cndmask_b32_dpp v86, v150, v154, vcc row_shl:4 row_mask:0xf bank_mask:0xf bound_ctrl:1\n\t"
      "v_cndmask_b32_dpp v87, v151, v155, vcc row_shl:4 row_mask:0xf bank_mask:0xf bound_ctrl:1\n\t"
      "s_mov_b32 vcc_lo, 0xff00ff\n\t"
      "s_mov_b32 vcc_hi, 0xff00ff\n\t"
      "v_cndmask_b32_dpp v132, v72, v64, vcc row_ror:8 row_mask:0xf bank_mask:0xf bound_ctrl:1\n\t"
      "v_cndmask_b32_dpp v133, v73, v65, vcc row_ror:8 row_mask:0xf bank_mask:0xf bound_ctrl:1\n\t"
      "v_cndmask_b32_dpp v134, v74, v66, vcc row_ror:8 row_mask:0xf bank_mask:0xf bound_ctrl:1\n\t"
      "v_cndmask_b32_dpp v135, v75, v67, vcc row_ror:8 row_mask:0xf bank_mask:0xf bound_ctrl:1\n\t"
      "v_cndmask_b32_dpp v136, v76, v68, vcc row_ror:8 row_mask:0xf bank_mask:0xf bound_ctrl:1\n\t"
      "v_cndmask_b32_dpp v137, v77, v69, vcc row_ror:8 row_mask:0xf bank_mask:0xf bound_ctrl:1\n\t"
      "v_cndmask_b32_dpp v138, v78, v70, vcc row_ror:8 row_mask:0xf bank_mask:0xf bound_ctrl:1\n\t"
      "v_cndmask_b32_dpp v139, v79, v71, vcc row_ror:8 row_mask:0xf bank_mask:0xf bound_ctrl:1\n\t"
      "v_mov_b32 v148, v80\n\t"
      "v_mov_b32 v149, v81\n\t"
      "v_mov_b32 v150, v82\n\t"
      "v_mov_b32 v151, v83\n\t"
      "v_mov_b32 v152, v84\n\t"
      "v_mov_b32 v154, v86\n\t"
      "v_mov_b32 v155, v87\n\t"
      "s_not_b64 vcc, vcc\n\t"
      "v_cndmask_b32_dpp v140, v64, v72, vcc row_ror:8 row_mask:0xf bank_mask:0xf bound_ctrl:1\n\t"
      "v_cndmask_b32_dpp v141, v65, v73, vcc row_ror:8 row_mask:0xf bank_mask:0xf bound_ctrl:1\n\t"
      "v_cndmask_b32_dpp v142, v66, v74, vcc row_ror:8 row_mask:0xf bank_mask:0xf bound_ctrl:1\n\t"
      "v_cndmask_b32_dpp v143, v67, v75, vcc row_ror:8 row_mask:0xf bank_mask:0xf bound_ctrl:1\n\t"
      "v_cndmask_b32_dpp v144, v68, v76, vcc row_ror:8 row_mask:0xf bank_mask:0xf bound_ctrl:1\n\t"
      "v_cndmask_b32_dpp v145, v69, v77, vcc row_ror:8 row_mask:0xf bank_mask:0xf bound_ctrl:1\n\t"
      "v_cndmask_b32_dpp v146, v70, v78, vcc row_ror:8 row_mask:0xf bank_mask:0xf bound_ctrl:1\n\t"
      "v_cndmask_b32_dpp v147, v71, v79, vcc row_ror:8 row_mask:0xf bank_mask:0xf bound_ctrl:1\n\t"
      "v_mov_b32_dpp v156, v80 row_ror:8 row_mask:0xf bank_mask:0xf bound_ctrl:1\n\t"
      "v_mov_b32_dpp v157, v81 row_ror:8 row_mask:0xf bank_mask:0xf bound_ctrl:1\n\t"
      "v_mov_b32_dpp v158, v82 row_ror:8 row_mask:0xf bank_mask:0xf bound_ctrl:1\n\t"
      "v_mov_b32_dpp v159, v83 row_ror:8 row_mask:0xf bank_mask:0xf bound_ctrl:1\n\t"
      "v_mov_b32_dpp v160, v84 row_ror:8 row_mask:0xf bank_mask:0xf bound_ctrl:1\n\t"
      "v_mov_b32_dpp v161, v153 row_ror:8 row_mask:0xf bank_mask:0xf bound_ctrl:1\n\t"
      "v_mov_b32_dpp v162, v86 row_ror:8 row_mask:0xf bank_mask:0xf bound_ctrl:1\n\t"
      "v_mov_b32_dpp v163, v87 row_ror:8 row_mask:0xf bank_mask:0xf bound_ctrl:1\n\t"
      "v_permlane16_swap_b32 v132, v148\n\t"
      "v_permlane16_swap_b32 v133, v149\n\t"
      "v_permlane16_swap_b32 v134, v150\n\t"
      "v_permlane16_swap_b32 v135, v151\n\t"
      "v_permlane16_swap_b32 v136, v152\n\t"
      "v_permlane16_swap_b32 v137, v153\n\t"
      "v_permlane16_swap_b32 v138, v154\n\t"
      "v_permlane16_swap_b32 v139, v155\n\t"
      "v_permlane16_swap_b32 v140, v156\n\t"
      "v_permlane16_swap_b32 v141, v157\n\t"
      "v_permlane16_swap_b32 v142, v158\n\t"
      "v_permlane16_swap_b32 v143, v159\n\t"
      "v_permlane16_swap_b32 v144, v160\n\t"
      "v_permlane16_swap_b32 v145, v161\n\t"
      "v_permlane16_swap_b32 v146, v162\n\t"
      "v_permlane16_swap_b32 v147, v163\n\t"
      "s_barrier\n\t"
      "global_load_dwordx2 v[242:243], v12, s[76:77] sc1\n\t"
      "v_mfma_f32_32x32x16_bf16 v[64:79], v[132:135], v[100:103], 0\n\t"
      "v_mfma_f32_32x32x16_bf16 v[64:79], v[136:139], v[104:107], v[64:79]\n\t"
      "v_mfma_f32_32x32x16_bf16 v[64:79], v[140:143], v[108:111], v[64:79]\n\t"
      "v_mfma_f32_32x32x16_bf16 v[64:79], v[144:147], v[112:115], v[64:79]\n\t"
      "v_mfma_f32_32x32x16_bf16 v[64:79], v[148:151], v[116:119], v[64:79]\n\t"
      "v_mfma_f32_32x32x16_bf16 v[64:79], v[152:155], v[120:123], v[64:79]\n\t"
      "v_mfma_f32_32x32x16_bf16 v[64:79], v[156:159], v[124:127], v[64:79]\n\t"
      "v_mfma_f32_32x32x16_bf16 v[64:79], v[160:163], v[128:131], v[64:79]\n\t"
      "s_and_b32 s61, s56, 2\n\t"
      "s_mul_i32 s61, s61, 8704\n\t"
      "v_lshlrev_b32 v22, 5, v1\n\t"
      "v_add_u32 v24, 512, v32\n\t"
      "v_cmp_gt_u32 vcc, 32, v1\n\t"
      "v_lshlrev_b32 v25, 3, v1\n\t"
      "v_add_u32 v25, 1536, v25\n\t"
      "v_cndmask_b32 v232, v24, v22, vcc\n\t"
      "s_cmp_eq_u32 s57, 0\n\t"
      "s_cselect_b64 s[66:67], -1, 0\n\t"
      "v_cndmask_b32 v232, v25, v232, s[66:67]\n\t"
      "v_add_u32 v232, s61, v232\n\t"
      "s_lshl_b32 s62, s57, 8\n\t"
      "s_add_u32 s62, s62, s61\n\t"
      "s_add_u32 s62, s62, 2048\n\t"
      "v_lshlrev_b32 v233, 2, v1\n\t"
      "v_add_u32 v233, s62, v233\n\t"
      "v_add_u32 v234, 8704, v233\n\t"
      "v_mov_b32 v22, 1\n\t"
      "s_barrier\n\t"
      "s_mov_b32 s64, 200000\n\t"
      "101:\n\t"
      "s_waitcnt vmcnt(0)\n\t"
      "v_cmp_ne_u64 vcc, v[242:243], v[10:11]\n\t"
      "s_nop 4\n\t"
      "s_cbranch_vccz 102f\n\t"
      "s_sub_u32 s64, s64, 1\n\t"
      "s_cbranch_scc1 102f\n\t"
      "s_sleep 4\n\t"
      "global_load_dwordx2 v[242:243], v12, s[76:77] sc1\n\t"
      "s_branch 101b\n\t"
      "102:\n\t"
      "buffer_load_dwordx4 v[164:167], v13, s[44:47], 0 offen sc1\n\t"
      "buffer_load_dwordx2 v[196:197], v13, s[44:47], 0 offen offset:16 sc1\n\t"
      "buffer_load_dwordx4 v[180:183], v13, s[44:47], 0 offen offset:1024 sc1\n\t"
      "buffer_load_dwordx2 v[198:199], v13, s[44:47], 0 offen offset:1040 sc1\n\t"
      "s_setprio 0\n\t"
      "ds_write_b128 v15, v[64:67]\n\t"
      "ds_write_b64 v16, v[68:69]\n\t"
      "ds_write_b64 v17, v[70:71]\n\t"
      "ds_write2_b32 v18, v72, v73 offset1:32\n\t"
      "ds_write2_b32 v18, v74, v75 offset0:64 offset1:96\n\t"
      "ds_write_b32 v19, v22\n\t"
      "s_mov_b32 s64, 0x400000\n\t"
      "110:\n\t"
      "ds_read_b32 v25, v20\n\t"
      "s_waitcnt lgkmcnt(0)\n\t"
      "v_cmp_gt_u32 vcc, 1, v25\n\t"
      "s_sub_u32 s64, s64, 1\n\t"
      "s_cbranch_scc1 111f\n\t"
      "s_nop 2\n\t"
      "s_cbranch_vccnz 110b\n\t"
      "111:\n\t"
      "ds_read2_b32 v[216:217], v233 offset1:1\n\t"
      "ds_read2_b32 v[218:219], v234 offset1:1\n\t"
      "s_cmp_lg_u32 s57, 0\n\t"
      "s_cbranch_scc1 120f\n\t"
      "ds_read_b128 v[200:203], v232\n\t"
      "ds_read_b128 v[204:207], v232 offset:16\n\t"
      "ds_read_b128 v[208:211], v232 offset:8704\n\t"
      "ds_read_b128 v[212:215], v232 offset:8720\n\t"
      "v_cmp_gt_u32 vcc, 32, v1\n\t"
      "s_waitcnt lgkmcnt(0)\n\t"
      "v_pk_add_f32 v[200:201], v[200:201], v[208:209]\n\t"
      "v_pk_add_f32 v[202:203], v[202:203], v[210:211]\n\t"
      "v_pk_add_f32 v[204:205], v[204:205], v[212:213]\n\t"
      "v_pk_add_f32 v[206:207], v[206:207], v[214:215]\n\t"
      "v_cndmask_b32 v209, v200, v201, vcc\n\t"
      "v_cndmask_b32 v210, v201, v202, vcc\n\t"
      "v_cndmask_b32 v211, v202, v203, vcc\n\t"
      "v_cndmask_b32 v212, v202, v204, vcc\n\t"
      "v_cndmask_b32 v213, v202, v205, vcc\n\t"
      "v_cndmask_b32 v214, v203, v206, vcc\n\t"
      "v_cndmask_b32 v215, v204, v207, vcc\n\t"
      "v_cvt_pk_bf16_f32 v220, v215, v214\n\t"
      "v_cvt_pk_bf16_f32 v221, v213, v212\n\t"
      "v_cvt_pk_bf16_f32 v222, v211, v210\n\t"
      "v_cvt_pk_bf16_f32 v223, v209, v200\n\t"
      "ds_write_b128 v33, v[220:223]\n\t"
      "s_waitcnt vmcnt(3)\n\t"
      "v_readfirstlane_b32 s70, v164\n\t"
      "v_readfirstlane_b32 s71, v165\n\t"
      "v_mov_b32 v224, 0\n\t"
      "v_mov_b32 v225, 0\n\t"
      "v_mov_b32 v226, 0\n\t"
      "v_mov_b32 v227, 0\n\t"
      "v_mov_b32 v228, 0\n\t"
      "v_mov_b32 v229, 0\n\t"
      "v_mov_b32 v230, 0\n\t"
      "v_mov_b32 v231, 0\n\t"
      "v_mov_b32 v232, 0\n\t"
      "v_mov_b32 v233, 0\n\t"
      "v_mov_b32 v234, 0\n\t"
      "v_mov_b32 v235, 0\n\t"
      "v_mov_b32 v236, 0\n\t"
      "v_mov_b32 v237, 0\n\t"
      "v_mov_b32 v238, 0\n\t"
      "v_mov_b32 v239, 0\n\t"
      "s_lshl_b32 s61, s70, 16\n\t"
      "s_lshr_b32 s62, s70, 16\n\t"
      "s_lshl_b32 s63, s71, 16\n\t"
      "s_or_b32 s62, s62, s63\n\t"
      "v_writelane_b32 v231, s61, 1\n\t"
      "v_writelane_b32 v235, s70, 1\n\t"
      "v_writelane_b32 v238, s61, 1\n\t"
      "v_writelane_b32 v239, s62, 1\n\t"
      "v_subrev_u32 v24, 32, v14\n\t"
      "s_mov_b64 s[66:67], exec\n\t"
      "s_mov_b64 exec, 3\n\t"
      "s_nop 0\n\t"
      "ds_write_b128 v24, v[224:227] offset:0\n\t"
      "ds_write_b128 v24, v[228:231] offset:4160\n\t"
      "ds_write_b128 v24, v[232:235] offset:8320\n\t"
      "ds_write_b128 v24, v[236:239] offset:12480\n\t"
      "s_mov_b64 exec, s[66:67]\n\t"
      "s_branch 121f\n\t"
      "120:\n\t"
      "ds_read_b64 v[200:201], v232\n\t"
      "ds_read_b32 v202, v232 offset:8\n\t"
      "ds_read_b64 v[204:205], v232 offset:8704\n\t"
      "ds_read_b32 v206, v232 offset:8712\n\t"
      "s_waitcnt lgkmcnt(0)\n\t"
      "v_pk_add_f32 v[200:201], v[200:201], v[204:205]\n\t"
      "v_add_f32 v202, v202, v206\n\t"
      "v_mov_b32 v224, 0\n\t"
      "v_mov_b32 v225, 0\n\t"
      "v_mov_b32 v226, 0\n\t"
      "v_mov_b32 v227, 0\n\t"
      "v_cvt_pk_bf16_f32 v220, v202, v202\n\t"
      "v_cvt_pk_bf16_f32 v221, v201, v201\n\t"
      "v_cvt_pk_bf16_f32 v222, v201, v200\n\t"
      "v_cvt_pk_bf16_f32 v223, v200, v200\n\t"
      "v_subrev_u32 v24, 1024, v33\n\t"
      "v_lshl_add_u32 v25, v32, 1, v33\n\t"
      "ds_write_b128 v24, v[220:223]\n\t"
      "s_mov_b64 s[66:67], exec\n\t"
      "s_mov_b64 exec, 1073741823\n\t"
      "s_nop 0\n\t"
      "ds_write_b128 v25, v[224:227] offset:16\n\t"
      "s_mov_b64 exec, s[66:67]\n\t"
      "121:\n\t"
      "v_pk_add_f32 v[216:217], v[216:217], v[218:219]\n\t"
      "v_and_b32 v22, 1, v1\n\t"
      "v_cmp_eq_u32 vcc, 0, v22\n\t"
      "s_lshl_b32 s61, s57, 10\n\t"
      "s_add_u32 s61, s61, 2048\n\t"
      "v_cvt_pk_bf16_f32 v240, v217, v217\n\t"
      "v_cvt_pk_bf16_f32 v241, v217, v216\n\t"
      "v_cvt_pk_bf16_f32 v242, v216, v216\n\t"
      "v_cndmask_b32 v241, v240, v241, vcc\n\t"
      "v_mov_b32 v243, v242\n\t"
      "v_subrev_u32 v24, s61, v33\n\t"
      "v_mov_b32 v22, 2\n\t"
      "ds_write_b128 v24, v[240:243]\n\t"
      "s_waitcnt vmcnt(2)\n\t"
      "ds_write_b128 v14, v[164:167] offset:0\n\t"
      "v_alignbit_b32 v168, v165, v164, 16\n\t"
      "v_alignbit_b32 v169, v166, v165, 16\n\t"
      "v_alignbit_b32 v170, v167, v166, 16\n\t"
      "v_alignbit_b32 v171, v196, v167, 16\n\t"
      "ds_write_b128 v14, v[168:171] offset:4160\n\t"
      "v_mov_b32 v172, v165\n\t"
      "v_mov_b32 v173, v166\n\t"
      "v_mov_b32 v174, v167\n\t"
      "v_mov_b32 v175, v196\n\t"
      "ds_write_b128 v14, v[172:175] offset:8320\n\t"
      "v_alignbit_b32 v176, v166, v165, 16\n\t"
      "v_alignbit_b32 v177, v167, v166, 16\n\t"
      "v_alignbit_b32 v178, v196, v167, 16\n\t"
      "v_alignbit_b32 v179, v197, v196, 16\n\t"
      "ds_write_b128 v14, v[176:179] offset:12480\n\t"
      "s_waitcnt vmcnt(0)\n\t"
      "ds_write_b128 v14, v[180:183] offset:1024\n\t"
      "v_alignbit_b32 v184, v181, v180, 16\n\t"
      "v_alignbit_b32 v185, v182, v181, 16\n\t"
      "v_alignbit_b32 v186, v183, v182, 16\n\t"
      "v_alignbit_b32 v187, v198, v183, 16\n\t"
      "ds_write_b128 v14, v[184:187] offset:5184\n\t"
      "v_mov_b32 v188, v181\n\t"
      "v_mov_b32 v189, v182\n\t"
      "v_mov_b32 v190, v183\n\t"
      "v_mov_b32 v191, v198\n\t"
      "ds_write_b128 v14, v[188:191] offset:9344\n\t"
      "v_alignbit_b32 v192, v182, v181, 16\n\t"
      "v_alignbit_b32 v193, v183, v182, 16\n\t"
      "v_alignbit_b32 v194, v198, v183, 16\n\t"
      "v_alignbit_b32 v195, v199, v198, 16\n\t"
      "ds_write_b128 v14, v[192:195] offset:13504\n\t"
      "s_waitcnt lgkmcnt(0)\n\t"
      "s_barrier\n\t"
      "s_setprio 2\n\t"
      "ds_read_b128 v[64:67], v28 offset:3776\n\t"
      "ds_read_b64 v[96:97], v29 offset:192\n\t"
      "ds_read_b64 v[98:99], v29 offset:200\n\t"
      "ds_read_b128 v[68:71], v28 offset:3264\n\t"
      "ds_read_b64 v[100:101], v29 offset:704\n\t"
      "ds_read_b64 v[102:103], v29 offset:712\n\t"
      "ds_read_b128 v[72:75], v28 offset:2752\n\t"
      "ds_read_b64 v[104:105], v29 offset:1216\n\t"
      "ds_read_b64 v[106:107], v29 offset:1224\n\t"
      "ds_read_b128 v[76:79], v28 offset:2240\n\t"
      "ds_read_b64 v[108:109], v29 offset:1728\n\t"
      "ds_read_b64 v[110:111], v29 offset:1736\n\t"
      "ds_read_b128 v[80:83], v28 offset:1728\n\t"
      "ds_read_b64 v[112:113], v29 offset:2240\n\t"
      "ds_read_b64 v[114:115], v29 offset:2248\n\t"
      "s_waitcnt lgkmcnt(12)\n\t"
      "v_mfma_f32_16x16x32_bf16 v[192:195], v[96:99], v[64:67], 0\n\t"
      "ds_read_b128 v[84:87], v28 offset:1216\n\t"
      "ds_read_b64 v[116:117], v29 offset:2752\n\t"
      "s_waitcnt lgkmcnt(11)\n\t"
      "v_mfma_f32_16x16x32_bf16 v[196:199], v[100:103], v[64:67], 0\n\t"
      "ds_read_b64 v[118:119], v29 offset:2760\n\t"
      "v_mfma_f32_16x16x32_bf16 v[196:199], v[96:99], v[68:71], v[196:199]\n\t"
      "ds_read_b128 v[88:91], v28 offset:704\n\t"
      "v_mfma_f32_16x16x32_bf16 v[200:203], v[100:103], v[68:71], 0\n\t"
      "ds_read_b64 v[120:121], v29 offset:3264\n\t"
      "s_waitcnt lgkmcnt(11)\n\t"
      "v_mfma_f32_16x16x32_bf16 v[200:203], v[104:107], v[64:67], v[200:203]\n\t"
      "ds_read_b64 v[122:123], v29 offset:3272\n\t"
      "v_mfma_f32_16x16x32_bf16 v[200:203], v[96:99], v[72:75], v[200:203]\n\t"
      "ds_read_b128 v[92:95], v28 offset:192\n\t"
      "v_mfma_f32_16x16x32_bf16 v[204:207], v[104:107], v[68:71], 0\n\t"
      "ds_read_b64 v[124:125], v29 offset:3776\n\t"
      "v_mfma_f32_16x16x32_bf16 v[204:207], v[100:103], v[72:75], v[204:207]\n\t"
      "ds_read_b64 v[126:127], v29 offset:3784\n\t"
      "v_mfma_f32_16x16x32_bf16 v[208:211], v[104:107], v[72:75], 0\n\t"
      "ds_read_b128 v[128:131], v28 offset:3712\n\t"
      "s_waitcnt lgkmcnt(13)\n\t"
      "v_mfma_f32_16x16x32_bf16 v[204:207], v[108:111], v[64:67], v[204:207]\n\t"
      "ds_read_b64 v[160:161], v29 offset:128\n\t"
      "v_mfma_f32_16x16x32_bf16 v[204:207], v[96:99], v[76:79], v[204:207]\n\t"
      "ds_read_b64 v[162:163], v29 offset:136\n\t"
      "ds_read_b128 v[132:135], v28 offset:3200\n\t"
      "v_mfma_f32_16x16x32_bf16 v[208:211], v[108:111], v[68:71], v[208:211]\n\t"
      "ds_read_b64 v[164:165], v29 offset:640\n\t"
      "v_mfma_f32_16x16x32_bf16 v[208:211], v[100:103], v[76:79], v[208:211]\n\t"
      "ds_read_b64 v[166:167], v29 offset:648\n\t"
      "v_mfma_f32_16x16x32_bf16 v[212:215], v[108:111], v[72:75], 0\n\t"
      "ds_read_b128 v[136:139], v28 offset:2688\n\t"
      "v_mfma_f32_16x16x32_bf16 v[212:215], v[104:107], v[76:79], v[212:215]\n\t"
      "ds_read_b64 v[168:169], v29 offset:1152\n\t"
      "v_mfma_f32_16x16x32_bf16 v[216:219], v[108:111], v[76:79], 0\n\t"
      "ds_read_b64 v[170:171], v29 offset:1160\n\t"
      "s_waitcnt lgkmcnt(15)\n\t"
      "v_mfma_f32_16x16x32_bf16 v[208:211], v[112:115], v[64:67], v[208:211]\n\t"
      "ds_read_b128 v[140:143], v28 offset:2176\n\t"
      "v_mfma_f32_16x16x32_bf16 v[208:211], v[96:99], v[80:83], v[208:211]\n\t"
      "ds_read_b64 v[172:173], v29 offset:1664\n\t"
      "v_mfma_f32_16x16x32_bf16 v[212:215], v[112:115], v[68:71], v[212:215]\n\t"
      "ds_read_b64 v[174:175], v29 offset:1672\n\t"
      "v_mfma_f32_16x16x32_bf16 v[212:215], v[100:103], v[80:83], v[212:215]\n\t"
      "ds_read_b128 v[144:147], v28 offset:1664\n\t"
      "v_mfma_f32_16x16x32_bf16 v[216:219], v[112:115], v[72:75], v[216:219]\n\t"
      "ds_read_b64 v[176:177], v29 offset:2176\n\t"
      "ds_read_b64 v[178:179], v29 offset:2184\n\t"
      "v_mfma_f32_16x16x32_bf16 v[216:219], v[104:107], v[80:83], v[216:219]\n\t"
      "ds_read_b128 v[148:151], v28 offset:1152\n\t"
      "v_mfma_f32_16x16x32_bf16 v[220:223], v[112:115], v[76:79], 0\n\t"
      "ds_read_b64 v[180:181], v29 offset:2688\n\t"
      "v_mfma_f32_16x16x32_bf16 v[220:223], v[108:111], v[80:83], v[220:223]\n\t"
      "ds_read_b64 v[182:183], v29 offset:2696\n\t"
      "v_mfma_f32_16x16x32_bf16 v[212:215], v[116:119], v[64:67], v[212:215]\n\t"
      "ds_read_b128 v[152:155], v28 offset:640\n\t"
      "v_mfma_f32_16x16x32_bf16 v[212:215], v[96:99], v[84:87], v[212:215]\n\t"
      "ds_read_b64 v[184:185], v29 offset:3200\n\t"
      "v_mfma_f32_16x16x32_bf16 v[216:219], v[116:119], v[68:71], v[216:219]\n\t"
      "ds_read_b64 v[186:187], v29 offset:3208\n\t"
      "v_mfma_f32_16x16x32_bf16 v[216:219], v[100:103], v[84:87], v[216:219]\n\t"
      "ds_read_b128 v[156:159], v28 offset:128\n\t"
      "v_mfma_f32_16x16x32_bf16 v[220:223], v[116:119], v[72:75], v[220:223]\n\t"
      "ds_read_b64 v[188:189], v29 offset:3712\n\t"
      "v_mfma_f32_16x16x32_bf16 v[220:223], v[104:107], v[84:87], v[220:223]\n\t"
      "ds_read_b64 v[190:191], v29 offset:3720\n\t"
      "s_waitcnt lgkmcnt(15)\n\t"
      "v_mfma_f32_16x16x32_bf16 v[216:219], v[120:123], v[64:67], v[216:219]\n\t"
      "v_mfma_f32_16x16x32_bf16 v[216:219], v[96:99], v[88:91], v[216:219]\n\t"
      "v_mfma_f32_16x16x32_bf16 v[220:223], v[120:123], v[68:71], v[220:223]\n\t"
      "v_mfma_f32_16x16x32_bf16 v[220:223], v[100:103], v[88:91], v[220:223]\n\t"
      "v_mfma_f32_16x16x32_bf16 v[220:223], v[124:127], v[64:67], v[220:223]\n\t"
      "v_mfma_f32_16x16x32_bf16 v[220:223], v[96:99], v[92:95], v[220:223]\n\t"
      "s_waitcnt lgkmcnt(0)\n\t"
      "v_mfma_f32_16x16x32_bf16 v[192:195], v[160:163], v[128:131], v[192:195]\n\t"
      "ds_read_b128 v[64:67], v28 offset:3648\n\t"
      "v_mfma_f32_16x16x32_bf16 v[196:199], v[164:167], v[128:131], v[196:199]\n\t"
      "ds_read_b64 v[96:97], v29 offset:64\n\t"
      "v_mfma_f32_16x16x32_bf16 v[196:199], v[160:163], v[132:135], v[196:199]\n\t"
      "ds_read_b64 v[98:99], v29 offset:72\n\t"
      "v_mfma_f32_16x16x32_bf16 v[200:203], v[164:167], v[132:135], v[200:203]\n\t"
      "ds_read_b128 v[68:71], v28 offset:3136\n\t"
      "v_mfma_f32_16x16x32_bf16 v[200:203], v[168:171], v[128:131], v[200:203]\n\t"
      "v_mfma_f32_16x16x32_bf16 v[200:203], v[160:163], v[136:139], v[200:203]\n\t"
      "ds_read_b64 v[100:101], v29 offset:576\n\t"
      "v_mfma_f32_16x16x32_bf16 v[204:207], v[168:171], v[132:135], v[204:207]\n\t"
      "ds_read_b64 v[102:103], v29 offset:584\n\t"
      "v_mfma_f32_16x16x32_bf16 v[204:207], v[164:167], v[136:139], v[204:207]\n\t"
      "ds_read_b128 v[72:75], v28 offset:2624\n\t"
      "v_mfma_f32_16x16x32_bf16 v[208:211], v[168:171], v[136:139], v[208:211]\n\t"
      "ds_read_b64 v[104:105], v29 offset:1088\n\t"
      "v_mfma_f32_16x16x32_bf16 v[204:207], v[172:175], v[128:131], v[204:207]\n\t"
      "v_mfma_f32_16x16x32_bf16 v[204:207], v[160:163], v[140:143], v[204:207]\n\t"
      "ds_read_b64 v[106:107], v29 offset:1096\n\t"
      "v_mfma_f32_16x16x32_bf16 v[208:211], v[172:175], v[132:135], v[208:211]\n\t"
      "ds_read_b128 v[76:79], v28 offset:2112\n\t"
      "v_mfma_f32_16x16x32_bf16 v[208:211], v[164:167], v[140:143], v[208:211]\n\t"
      "ds_read_b64 v[108:109], v29 offset:1600\n\t"
      "v_mfma_f32_16x16x32_bf16 v[212:215], v[172:175], v[136:139], v[212:215]\n\t"
      "ds_read_b64 v[110:111], v29 offset:1608\n\t"
      "v_mfma_f32_16x16x32_bf16 v[212:215], v[168:171], v[140:143], v[212:215]\n\t"
      "v_mfma_f32_16x16x32_bf16 v[216:219], v[172:175], v[140:143], v[216:219]\n\t"
      "ds_read_b128 v[80:83], v28 offset:1600\n\t"
      "v_mfma_f32_16x16x32_bf16 v[208:211], v[176:179], v[128:131], v[208:211]\n\t"
      "ds_read_b64 v[112:113], v29 offset:2112\n\t"
      "v_mfma_f32_16x16x32_bf16 v[208:211], v[160:163], v[144:147], v[208:211]\n\t"
      "ds_read_b64 v[114:115], v29 offset:2120\n\t"
      "v_mfma_f32_16x16x32_bf16 v[212:215], v[176:179], v[132:135], v[212:215]\n\t"
      "ds_read_b128 v[84:87], v28 offset:1088\n\t"
      "v_mfma_f32_16x16x32_bf16 v[212:215], v[164:167], v[144:147], v[212:215]\n\t"
      "v_mfma_f32_16x16x32_bf16 v[216:219], v[176:179], v[136:139], v[216:219]\n\t"
      "ds_read_b64 v[116:117], v29 offset:2624\n\t"
      "v_mfma_f32_16x16x32_bf16 v[216:219], v[168:171], v[144:147], v[216:219]\n\t"
      "ds_read_b64 v[118:119], v29 offset:2632\n\t"
      "v_mfma_f32_16x16x32_bf16 v[220:223], v[176:179], v[140:143], v[220:223]\n\t"
      "ds_read_b128 v[88:91], v28 offset:576\n\t"
      "v_mfma_f32_16x16x32_bf16 v[220:223], v[172:175], v[144:147], v[220:223]\n\t"
      "ds_read_b64 v[120:121], v29 offset:3136\n\t"
      "v_mfma_f32_16x16x32_bf16 v[212:215], v[180:183], v[128:131], v[212:215]\n\t"
      "v_mfma_f32_16x16x32_bf16 v[212:215], v[160:163], v[148:151], v[212:215]\n\t"
      "ds_read_b64 v[122:123], v29 offset:3144\n\t"
      "v_mfma_f32_16x16x32_bf16 v[216:219], v[180:183], v[132:135], v[216:219]\n\t"
      "ds_read_b128 v[92:95], v28 offset:64\n\t"
      "v_mfma_f32_16x16x32_bf16 v[216:219], v[164:167], v[148:151], v[216:219]\n\t"
      "ds_read_b64 v[124:125], v29 offset:3648\n\t"
      "v_mfma_f32_16x16x32_bf16 v[220:223], v[180:183], v[136:139], v[220:223]\n\t"
      "ds_read_b64 v[126:127], v29 offset:3656\n\t"
      "v_mfma_f32_16x16x32_bf16 v[220:223], v[168:171], v[148:151], v[220:223]\n\t"
      "v_mfma_f32_16x16x32_bf16 v[216:219], v[184:187], v[128:131], v[216:219]\n\t"
      "v_mfma_f32_16x16x32_bf16 v[216:219], v[160:163], v[152:155], v[216:219]\n\t"
      "v_mfma_f32_16x16x32_bf16 v[220:223], v[184:187], v[132:135], v[220:223]\n\t"
      "v_mfma_f32_16x16x32_bf16 v[220:223], v[164:167], v[152:155], v[220:223]\n\t"
      "v_mfma_f32_16x16x32_bf16 v[220:223], v[188:191], v[128:131], v[220:223]\n\t"
      "v_mfma_f32_16x16x32_bf16 v[220:223], v[160:163], v[156:159], v[220:223]\n\t"
      "s_waitcnt lgkmcnt(0)\n\t"
      "v_mfma_f32_16x16x32_bf16 v[192:195], v[96:99], v[64:67], v[192:195]\n\t"
      "ds_read_b128 v[128:131], v28 offset:3584\n\t"
      "v_mfma_f32_16x16x32_bf16 v[196:199], v[100:103], v[64:67], v[196:199]\n\t"
      "ds_read_b64 v[160:161], v29 offset:0\n\t"
      "v_mfma_f32_16x16x32_bf16 v[196:199], v[96:99], v[68:71], v[196:199]\n\t"
      "ds_read_b64 v[162:163], v29 offset:8\n\t"
      "v_mfma_f32_16x16x32_bf16 v[200:203], v[100:103], v[68:71], v[200:203]\n\t"
      "ds_read_b128 v[132:135], v28 offset:3072\n\t"
      "v_mfma_f32_16x16x32_bf16 v[200:203], v[104:107], v[64:67], v[200:203]\n\t"
      "v_mfma_f32_16x16x32_bf16 v[200:203], v[96:99], v[72:75], v[200:203]\n\t"
      "ds_read_b64 v[164:165], v29 offset:512\n\t"
      "v_mfma_f32_16x16x32_bf16 v[204:207], v[104:107], v[68:71], v[204:207]\n\t"
      "ds_read_b64 v[166:167], v29 offset:520\n\t"
      "v_mfma_f32_16x16x32_bf16 v[204:207], v[100:103], v[72:75], v[204:207]\n\t"
      "ds_read_b128 v[136:139], v28 offset:2560\n\t"
      "v_mfma_f32_16x16x32_bf16 v[208:211], v[104:107], v[72:75], v[208:211]\n\t"
      "ds_read_b64 v[168:169], v29 offset:1024\n\t"
      "v_mfma_f32_16x16x32_bf16 v[204:207], v[108:111], v[64:67], v[204:207]\n\t"
      "v_mfma_f32_16x16x32_bf16 v[204:207], v[96:99], v[76:79], v[204:207]\n\t"
      "ds_read_b64 v[170:171], v29 offset:1032\n\t"
      "v_mfma_f32_16x16x32_bf16 v[208:211], v[108:111], v[68:71], v[208:211]\n\t"
      "ds_read_b128 v[140:143], v28 offset:2048\n\t"
      "v_mfma_f32_16x16x32_bf16 v[208:211], v[100:103], v[76:79], v[208:211]\n\t"
      "ds_read_b64 v[172:173], v29 offset:1536\n\t"
      "v_mfma_f32_16x16x32_bf16 v[212:215], v[108:111], v[72:75], v[212:215]\n\t"
      "ds_read_b64 v[174:175], v29 offset:1544\n\t"
      "v_mfma_f32_16x16x32_bf16 v[212:215], v[104:107], v[76:79], v[212:215]\n\t"
      "v_mfma_f32_16x16x32_bf16 v[216:219], v[108:111], v[76:79], v[216:219]\n\t"
      "ds_read_b128 v[144:147], v28 offset:1536\n\t"
      "v_mfma_f32_16x16x32_bf16 v[208:211], v[112:115], v[64:67], v[208:211]\n\t"
      "ds_read_b64 v[176:177], v29 offset:2048\n\t"
      "v_mfma_f32_16x16x32_bf16 v[208:211], v[96:99], v[80:83], v[208:211]\n\t"
      "ds_read_b64 v[178:179], v29 offset:2056\n\t"
      "v_mfma_f32_16x16x32_bf16 v[212:215], v[112:115], v[68:71], v[212:215]\n\t"
      "ds_read_b128 v[148:151], v28 offset:1024\n\t"
      "v_mfma_f32_16x16x32_bf16 v[212:215], v[100:103], v[80:83], v[212:215]\n\t"
      "v_mfma_f32_16x16x32_bf16 v[216:219], v[112:115], v[72:75], v[216:219]\n\t"
      "ds_read_b64 v[180:181], v29 offset:2560\n\t"
      "v_mfma_f32_16x16x32_bf16 v[216:219], v[104:107], v[80:83], v[216:219]\n\t"
      "ds_read_b64 v[182:183], v29 offset:2568\n\t"
      "v_mfma_f32_16x16x32_bf16 v[220:223], v[112:115], v[76:79], v[220:223]\n\t"
      "ds_read_b128 v[152:155], v28 offset:512\n\t"
      "v_mfma_f32_16x16x32_bf16 v[220:223], v[108:111], v[80:83], v[220:223]\n\t"
      "ds_read_b64 v[184:185], v29 offset:3072\n\t"
      "v_mfma_f32_16x16x32_bf16 v[212:215], v[116:119], v[64:67], v[212:215]\n\t"
      "v_mfma_f32_16x16x32_bf16 v[212:215], v[96:99], v[84:87], v[212:215]\n\t"
      "ds_read_b64 v[186:187], v29 offset:3080\n\t"
      "v_mfma_f32_16x16x32_bf16 v[216:219], v[116:119], v[68:71], v[216:219]\n\t"
      "ds_read_b128 v[156:159], v28 offset:0\n\t"
      "v_mfma_f32_16x16x32_bf16 v[216:219], v[100:103], v[84:87], v[216:219]\n\t"
      "ds_read_b64 v[188:189], v29 offset:3584\n\t"
      "v_mfma_f32_16x16x32_bf16 v[220:223], v[116:119], v[72:75], v[220:223]\n\t"
      "ds_read_b64 v[190:191], v29 offset:3592\n\t"
      "v_mfma_f32_16x16x32_bf16 v[220:223], v[104:107], v[84:87], v[220:223]\n\t"
      "v_mfma_f32_16x16x32_bf16 v[216:219], v[120:123], v[64:67], v[216:219]\n\t"
      "v_mfma_f32_16x16x32_bf16 v[216:219], v[96:99], v[88:91], v[216:219]\n\t"
      "v_mfma_f32_16x16x32_bf16 v[220:223], v[120:123], v[68:71], v[220:223]\n\t"
      "v_mfma_f32_16x16x32_bf16 v[220:223], v[100:103], v[88:91], v[220:223]\n\t"
      "v_mfma_f32_16x16x32_bf16 v[220:223], v[124:127], v[64:67], v[220:223]\n\t"
      "v_mfma_f32_16x16x32_bf16 v[220:223], v[96:99], v[92:95], v[220:223]\n\t"
      "s_waitcnt lgkmcnt(0)\n\t"
      "v_mfma_f32_16x16x32_bf16 v[192:195], v[160:163], v[128:131], v[192:195]\n\t"
      "ds_read_b64 v[36:37], v30 offset:0\n\t"
      "v_mfma_f32_16x16x32_bf16 v[196:199], v[164:167], v[128:131], v[196:199]\n\t"
      "ds_read_b64 v[38:39], v30 offset:512\n\t"
      "v_mfma_f32_16x16x32_bf16 v[196:199], v[160:163], v[132:135], v[196:199]\n\t"
      "ds_read_b64 v[40:41], v30 offset:1024\n\t"
      "v_mfma_f32_16x16x32_bf16 v[200:203], v[164:167], v[132:135], v[200:203]\n\t"
      "ds_read_b64 v[42:43], v30 offset:1536\n\t"
      "v_mfma_f32_16x16x32_bf16 v[200:203], v[168:171], v[128:131], v[200:203]\n\t"
      "v_mfma_f32_16x16x32_bf16 v[200:203], v[160:163], v[136:139], v[200:203]\n\t"
      "v_mfma_f32_16x16x32_bf16 v[204:207], v[168:171], v[132:135], v[204:207]\n\t"
      "v_mfma_f32_16x16x32_bf16 v[204:207], v[164:167], v[136:139], v[204:207]\n\t"
      "v_mfma_f32_16x16x32_bf16 v[208:211], v[168:171], v[136:139], v[208:211]\n\t"
      "v_mfma_f32_16x16x32_bf16 v[204:207], v[172:175], v[128:131], v[204:207]\n\t"
      "s_waitcnt lgkmcnt(0)\n\t"
      "v_mfma_f32_16x16x32_bf16 v[204:207], v[160:163], v[140:143], v[204:207]\n\t"
      "v_lshlrev_b32 v224, 16, v36\n\t"
      "v_mfma_f32_16x16x32_bf16 v[208:211], v[172:175], v[132:135], v[208:211]\n\t"
      "v_mfma_f32_16x16x32_bf16 v[208:211], v[164:167], v[140:143], v[208:211]\n\t"
      "v_and_b32 v225, 0xffff0000, v36\n\t"
      "v_mfma_f32_16x16x32_bf16 v[212:215], v[172:175], v[136:139], v[212:215]\n\t"
      "v_lshlrev_b32 v226, 16, v37\n\t"
      "v_mfma_f32_16x16x32_bf16 v[212:215], v[168:171], v[140:143], v[212:215]\n\t"
      "v_and_b32 v227, 0xffff0000, v37\n\t"
      "v_mfma_f32_16x16x32_bf16 v[216:219], v[172:175], v[140:143], v[216:219]\n\t"
      "v_lshlrev_b32 v228, 16, v38\n\t"
      "v_mfma_f32_16x16x32_bf16 v[208:211], v[176:179], v[128:131], v[208:211]\n\t"
      "v_and_b32 v229, 0xffff0000, v38\n\t"
      "v_mfma_f32_16x16x32_bf16 v[208:211], v[160:163], v[144:147], v[208:211]\n\t"
      "v_mfma_f32_16x16x32_bf16 v[212:215], v[176:179], v[132:135], v[212:215]\n\t"
      "v_lshlrev_b32 v230, 16, v39\n\t"
      "v_mfma_f32_16x16x32_bf16 v[212:215], v[164:167], v[144:147], v[212:215]\n\t"
      "v_and_b32 v231, 0xffff0000, v39\n\t"
      "v_mfma_f32_16x16x32_bf16 v[216:219], v[176:179], v[136:139], v[216:219]\n\t"
      "v_lshlrev_b32 v232, 16, v40\n\t"
      "v_mfma_f32_16x16x32_bf16 v[216:219], v[168:171], v[144:147], v[216:219]\n\t"
      "v_and_b32 v233, 0xffff0000, v40\n\t"
      "v_mfma_f32_16x16x32_bf16 v[220:223], v[176:179], v[140:143], v[220:223]\n\t"
      "v_lshlrev_b32 v234, 16, v41\n\t"
      "v_mfma_f32_16x16x32_bf16 v[220:223], v[172:175], v[144:147], v[220:223]\n\t"
      "v_mfma_f32_16x16x32_bf16 v[212:215], v[180:183], v[128:131], v[212:215]\n\t"
      "v_and_b32 v235, 0xffff0000, v41\n\t"
      "v_mfma_f32_16x16x32_bf16 v[212:215], v[160:163], v[148:151], v[212:215]\n\t"
      "v_lshlrev_b32 v236, 16, v42\n\t"
      "v_mfma_f32_16x16x32_bf16 v[216:219], v[180:183], v[132:135], v[216:219]\n\t"
      "v_and_b32 v237, 0xffff0000, v42\n\t"
      "v_mfma_f32_16x16x32_bf16 v[216:219], v[164:167], v[148:151], v[216:219]\n\t"
      "v_lshlrev_b32 v238, 16, v43\n\t"
      "v_mfma_f32_16x16x32_bf16 v[220:223], v[180:183], v[136:139], v[220:223]\n\t"
      "v_and_b32 v239, 0xffff0000, v43\n\t"
      "v_mfma_f32_16x16x32_bf16 v[220:223], v[168:171], v[148:151], v[220:223]\n\t"
      "v_mfma_f32_16x16x32_bf16 v[216:219], v[184:187], v[128:131], v[216:219]\n\t"
      "v_mfma_f32_16x16x32_bf16 v[216:219], v[160:163], v[152:155], v[216:219]\n\t"
      "v_mfma_f32_16x16x32_bf16 v[220:223], v[184:187], v[132:135], v[220:223]\n\t"
      "v_mfma_f32_16x16x32_bf16 v[220:223], v[164:167], v[152:155], v[220:223]\n\t"
      "v_mfma_f32_16x16x32_bf16 v[220:223], v[188:191], v[128:131], v[220:223]\n\t"
      "v_mfma_f32_16x16x32_bf16 v[220:223], v[160:163], v[156:159], v[220:223]\n\t"
      "s_setprio 2\n\t"
      "v_mov_b32 v22, 3\n\t"
      "s_cmp_lg_u32 s57, 0\n\t"
      "s_nop 4\n\t"
      "s_cbranch_scc1 122f\n\t"
      "ds_write_b128 v35, v[208:211] offset:0\n\t"
      "ds_write_b128 v35, v[212:215] offset:1024\n\t"
      "ds_write_b128 v35, v[216:219] offset:2048\n\t"
      "ds_write_b128 v35, v[220:223] offset:3072\n\t"
      "ds_write_b32 v19, v22\n\t"
      "v_pk_fma_f32 v[224:225], v[224:225], v[8:9], v[192:193] op_sel_hi:[1,0,1]\n\t"
      "v_pk_fma_f32 v[226:227], v[226:227], v[8:9], v[194:195] op_sel_hi:[1,0,1]\n\t"
      "v_pk_fma_f32 v[228:229], v[228:229], v[8:9], v[196:197] op_sel_hi:[1,0,1]\n\t"
      "v_pk_fma_f32 v[230:231], v[230:231], v[8:9], v[198:199] op_sel_hi:[1,0,1]\n\t"
      "v_pk_fma_f32 v[232:233], v[232:233], v[8:9], v[200:201] op_sel_hi:[1,0,1]\n\t"
      "v_pk_fma_f32 v[234:235], v[234:235], v[8:9], v[202:203] op_sel_hi:[1,0,1]\n\t"
      "v_pk_fma_f32 v[236:237], v[236:237], v[8:9], v[204:205] op_sel_hi:[1,0,1]\n\t"
      "v_pk_fma_f32 v[238:239], v[238:239], v[8:9], v[206:207] op_sel_hi:[1,0,1]\n\t"
      "s_branch 123f\n\t"
      "122:\n\t"
      "ds_write_b128 v35, v[192:195] offset:0\n\t"
      "ds_write_b128 v35, v[196:199] offset:1024\n\t"
      "ds_write_b128 v35, v[200:203] offset:2048\n\t"
      "ds_write_b128 v35, v[204:207] offset:3072\n\t"
      "ds_write_b32 v19, v22\n\t"
      "v_pk_fma_f32 v[224:225], v[224:225], v[8:9], v[208:209] op_sel_hi:[1,0,1]\n\t"
      "v_pk_fma_f32 v[226:227], v[226:227], v[8:9], v[210:211] op_sel_hi:[1,0,1]\n\t"
      "v_pk_fma_f32 v[228:229], v[228:229], v[8:9], v[212:213] op_sel_hi:[1,0,1]\n\t"
      "v_pk_fma_f32 v[230:231], v[230:231], v[8:9], v[214:215] op_sel_hi:[1,0,1]\n\t"
      "v_pk_fma_f32 v[232:233], v[232:233], v[8:9], v[216:217] op_sel_hi:[1,0,1]\n\t"
      "v_pk_fma_f32 v[234:235], v[234:235], v[8:9], v[218:219] op_sel_hi:[1,0,1]\n\t"
      "v_pk_fma_f32 v[236:237], v[236:237], v[8:9], v[220:221] op_sel_hi:[1,0,1]\n\t"
      "v_pk_fma_f32 v[238:239], v[238:239], v[8:9], v[222:223] op_sel_hi:[1,0,1]\n\t"
      "123:\n\t"
      "s_mov_b32 s64, 0x400000\n\t"
      "114:\n\t"
      "ds_read_b32 v25, v20\n\t"
      "s_waitcnt lgkmcnt(0)\n\t"
      "v_cmp_gt_u32 vcc, 3, v25\n\t"
      "s_sub_u32 s64, s64, 1\n\t"
      "s_cbranch_scc1 115f\n\t"
      "s_nop 2\n\t"
      "s_cbranch_vccnz 114b\n\t"
      "115:\n\t"
      "ds_read_b128 v[64:67], v34 offset:0\n\t"
      "ds_read_b128 v[68:71], v34 offset:1024\n\t"
      "ds_read_b128 v[72:75], v34 offset:2048\n\t"
      "ds_read_b128 v[76:79], v34 offset:3072\n\t"
      "s_waitcnt lgkmcnt(3)\n\t"
      "v_pk_add_f32 v[224:225], v[224:225], v[64:65]\n\t"
      "v_pk_add_f32 v[226:227], v[226:227], v[66:67]\n\t"
      "v_cvt_pk_bf16_f32 v80, v224, v225\n\t"
      "v_cvt_pk_bf16_f32 v81, v226, v227\n\t"
      "ds_write_b64 v31, v[80:81] offset:4096\n\t"
      "s_waitcnt lgkmcnt(3)\n\t"
      "v_pk_add_f32 v[228:229], v[228:229], v[68:69]\n\t"
      "v_pk_add_f32 v[230:231], v[230:231], v[70:71]\n\t"
      "v_cvt_pk_bf16_f32 v82, v228, v229\n\t"
      "v_cvt_pk_bf16_f32 v83, v230, v231\n\t"
      "ds_write_b64 v31, v[82:83] offset:4608\n\t"
      "ds_read_b128 v[88:91], v35 offset:4096\n\t"
      "s_waitcnt lgkmcnt(4)\n\t"
      "v_pk_add_f32 v[232:233], v[232:233], v[72:73]\n\t"
      "v_pk_add_f32 v[234:235], v[234:235], v[74:75]\n\t"
      "v_cvt_pk_bf16_f32 v84, v232, v233\n\t"
      "v_cvt_pk_bf16_f32 v85, v234, v235\n\t"
      "ds_write_b64 v31, v[84:85] offset:5120\n\t"
      "s_waitcnt lgkmcnt(4)\n\t"
      "v_pk_add_f32 v[236:237], v[236:237], v[76:77]\n\t"
      "v_pk_add_f32 v[238:239], v[238:239], v[78:79]\n\t"
      "v_cvt_pk_bf16_f32 v86, v236, v237\n\t"
      "v_cvt_pk_bf16_f32 v87, v238, v239\n\t"
      "ds_write_b64 v31, v[86:87] offset:5632\n\t"
      "ds_read_b128 v[92:95], v35 offset:5120\n\t"
      "s_lshl_b32 s61, s57, 12\n\t"
      "s_lshl3_add_u32 s61, s60, s61\n\t"
      "s_add_u32 s61, s61, 24576\n\t"
      "v_mov_b32 v22, s61\n\t"
      "s_waitcnt lgkmcnt(3)\n\t"
      "buffer_store_dwordx4 v[88:91], v13, s[48:51], 0 offen sc1\n\t"
      "s_waitcnt lgkmcnt(0)\n\t"
      "buffer_store_dwordx4 v[92:95], v13, s[48:51], 0 offen offset:1024 sc1\n\t"
      "s_waitcnt vmcnt(0)\n\t"
      "s_mov_b64 s[66:67], exec\n\t"
      "s_mov_b64 exec, 1\n\t"
      "s_nop 0\n\t"
      "global_store_dwordx2 v22, v[10:11], s[76:77] sc1\n\t"
      "s_mov_b64 exec, s[66:67]\n\t"
      "s_or_b32 s61, s59, s56\n\t"
      "s_cmp_lg_u32 s61, 0\n\t"
      "s_cbranch_scc1 109f\n\t"
      "v_lshrrev_b32 v22, 4, v1\n\t"
      "v_and_b32 v24, 15, v1\n\t"
      "v_lshlrev_b32 v22, 10, v22\n\t"
      "v_lshl_add_u32 v22, v24, 4, v22\n\t"
      "v_add_u32 v22, 8192, v22\n\t"
      "v_add_u32 v24, 4096, v22\n\t"
      "s_mov_b32 s64, 200000\n\t"
      "108:\n\t"
      "global_load_dwordx4 v[64:67], v22, s[76:77] sc1\n\t"
      "global_load_dwordx4 v[68:71], v24, s[76:77] sc1\n\t"
      "s_waitcnt vmcnt(0)\n\t"
      "v_cmp_ne_u64 vcc, v[64:65], v[10:11]\n\t"
      "v_cmp_ne_u64 s[66:67], v[66:67], v[10:11]\n\t"
      "s_nop 0\n\t"
      "s_or_b64 s[52:53], vcc, s[66:67]\n\t"
      "v_cmp_ne_u64 vcc, v[68:69], v[10:11]\n\t"
      "v_cmp_ne_u64 s[66:67], v[70:71], v[10:11]\n\t"
      "s_nop 0\n\t"
      "s_or_b64 s[52:53], s[52:53], vcc\n\t"
      "s_or_b64 s[52:53], s[52:53], s[66:67]\n\t"
      "s_cbranch_scc0 119f\n\t"
      "s_sub_u32 s64, s64, 1\n\t"
      "s_cbranch_scc1 119f\n\t"
      "s_sleep 16\n\t"
      "s_branch 108b\n\t"
      "119:\n\t"
      "v_mov_b32 v22, 0\n\t"
      "s_mov_b64 s[66:67], exec\n\t"
      "s_mov_b64 exec, 1\n\t"
      "s_nop 0\n\t"
      "global_store_dwordx2 v22, v[10:11], s[40:41] sc1\n\t"
      "s_mov_b64 exec, s[66:67]\n\t"
      "109:\n\t"
      "999:\n\t"
      :
      : [ka] "s"(ka), [bid] "s"(blockIdx.x), [tid] "v"(threadIdx.x)
      : "v1", "v2", "v3", "v4", "v5", "v6", "v7", "v8", "v9", "v10", "v11", "v12", "v13", "v14", "v15", "v16",
        "v17", "v18", "v19", "v20", "v21", "v22", "v23", "v24", "v25", "v26", "v27", "v28", "v29", "v30", "v31", "v32",
        "v33", "v34", "v35", "v36", "v37", "v38", "v39", "v40", "v41", "v42", "v43", "v44", "v45", "v46", "v47", "v48",
        "v49", "v50", "v51", "v52", "v53", "v54", "v55", "v56", "v57", "v58", "v59", "v60", "v61", "v62", "v63", "v64",
        "v65", "v66", "v67", "v68", "v69", "v70", "v71", "v72", "v73", "v74", "v75", "v76", "v77", "v78", "v79", "v80",
        "v81", "v82", "v83", "v84", "v85", "v86", "v87", "v88", "v89", "v90", "v91", "v92", "v93", "v94", "v95", "v96",
        "v97", "v98", "v99", "v100", "v101", "v102", "v103", "v104", "v105", "v106", "v107", "v108", "v109", "v110", "v111", "v112",
        "v113", "v114", "v115", "v116", "v117", "v118", "v119", "v120", "v121", "v122", "v123", "v124", "v125", "v126", "v127", "v128",
        "v129", "v130", "v131", "v132", "v133", "v134", "v135", "v136", "v137", "v138", "v139", "v140", "v141", "v142", "v143", "v144",
        "v145", "v146", "v147", "v148", "v149", "v150", "v151", "v152", "v153", "v154", "v155", "v156", "v157", "v158", "v159", "v160",
        "v161", "v162", "v163", "v164", "v165", "v166", "v167", "v168", "v169", "v170", "v171", "v172", "v173", "v174", "v175", "v176",
        "v177", "v178", "v179", "v180", "v181", "v182", "v183", "v184", "v185", "v186", "v187", "v188", "v189", "v190", "v191", "v192",
        "v193", "v194", "v195", "v196", "v197", "v198", "v199", "v200", "v201", "v202", "v203", "v204", "v205", "v206", "v207", "v208",
        "v209", "v210", "v211", "v212", "v213", "v214", "v215", "v216", "v217", "v218", "v219", "v220", "v221", "v222", "v223", "v224",
        "v225", "v226", "v227", "v228", "v229", "v230", "v231", "v232", "v233", "v234", "v235", "v236", "v237", "v238", "v239", "v240",
        "v241", "v242", "v243", "v244", "v245", "v246", "v247", "v248", "v249", "v250", "v251", "v252", "v253", "v254", "v255", "s20",
        "s21", "s22", "s23", "s24", "s25", "s26", "s27", "s28", "s29", "s30", "s31", "s33", "s34", "s35", "s36", "s37",
        "s38", "s39", "s40", "s41", "s42", "s43", "s44", "s45", "s46", "s47", "s48", "s49", "s50", "s51", "s52", "s53",
        "s54", "s55", "s56", "s57", "s58", "s59", "s60", "s61", "s62", "s63", "s64", "s65", "s66", "s67", "s68", "s69",
        "s70", "s71", "s72", "s73", "s74", "s75", "s76", "s77", "s78", "s79", "s80", "s81", "s82", "s83", "s84", "s85",
        "s86", "s87", "s88", "s89", "s90", "s91", "s92", "s93", "s94", "s95", "s96", "s97", "s98", "s99", "vcc", "scc",
        "memory");

}

extern "C" void kernel_launch(void* const* d_in, const int* in_sizes, int n_in, void* d_out, int out_size, void* d_ws, size_t ws_size,
                              hipStream_t stream) {
  Params p;
  p.u = (const u16*)d_in[0]; p.Are = (const u16*)d_in[1]; p.Aim = (const u16*)d_in[2]; p.Bre = (const u16*)d_in[3];
  p.Bim = (const u16*)d_in[4]; p.Cre = (const u16*)d_in[5]; p.Cim = (const u16*)d_in[6]; p.logdt = (const u16*)d_in[7];
  p.Dskip = (const u16*)d_in[8]; p.w1 = (const u16*)d_in[10]; p.b1 = (const u16*)d_in[11]; p.w2 = (const u16*)d_in[12];
  p.b2 = (const u16*)d_in[13]; p.gw = (const u16*)d_in[14]; p.out = (u16*)d_out; p.ws = (unsigned char*)d_ws;
  gated_mix_kernel<<<256, 512, 0, stream>>>(p);
}
```
